```python
import torch

def _softmax(x, axis=-1):
    return torch.softmax(x, dim=axis)
def _gelu(x):
    return torch.nn.functional.gelu(x, approximate="tanh")
def _silu(x):
    return torch.nn.functional.silu(x)
def _sigmoid(x):
    return torch.sigmoid(x)
def _layernorm(x, axis=-1, eps=1e-5):
    m = x.mean(dim=axis, keepdim=True)
    v = x.var(dim=axis, keepdim=True, unbiased=False)
    return (x - m) / torch.sqrt(v + eps)
def _scatter_add(tgt, idx, src, axis=0):
    return tgt.clone().index_add_(axis, idx, src)
def _segment_sum(src, seg, n):
    out = torch.zeros((n,) + src.shape[1:], dtype=src.dtype)
    return out.index_add_(0, seg, src)
def _pad(x, pw):
    flat = tuple(v for pair in reversed(pw) for v in pair)
    return torch.nn.functional.pad(x, flat)

kernel_name = 'standalone__kernel_systems_refs_featgraph__graph'

def setup_inputs():

    _w = (lambda a: (lambda t: t.to(torch.bfloat16) if t.is_floating_point() else t)(torch.as_tensor(a)))
    N, deg, D = 12500, 32, 128
    E = N * deg
    scale = D ** -0.5
    src = (lambda lo, hi, *s: torch.randint(lo, hi, s))(0, N, E).to(torch.int64)
    dst = (lambda lo, hi, *s: torch.randint(lo, hi, s))(0, N, E).to(torch.int64)
    return {
        "x": _w((lambda *s: torch.randn(*s, dtype=torch.bfloat16) * 0.8)(N, D)),
        "src": _w(src),
        "dst": _w(dst),
        "w_q": _w((lambda *s: torch.randn(*s, dtype=torch.bfloat16) * 0.8)(D, D) * scale),
        "w_v": _w((lambda *s: torch.randn(*s, dtype=torch.bfloat16) * 0.05)(D, D) * scale),
    }

def reference(x, src, dst, w_q, w_v):
    N, D = x.shape
    xs = x[src]
    xd = x[dst]
    q = xd @ w_q
    logits = torch.einsum('ed,ed->e', xs, q) * (float(D) ** -0.5)
    alpha = _sigmoid(logits)
    msg = alpha[:, None] * (xs @ w_v)
    return _segment_sum(msg, dst, N)
```

```cpp
#include <hip/hip_runtime.h>
#include <hip/hip_bf16.h>

typedef unsigned u32x4 __attribute__((ext_vector_type(4)));
typedef unsigned u32x2 __attribute__((ext_vector_type(2)));
typedef float f32x4 __attribute__((ext_vector_type(4)));
typedef __bf16 bf16x8 __attribute__((ext_vector_type(8)));
typedef __bf16 bf16x2 __attribute__((ext_vector_type(2)));

constexpr int NN = 12500, EE = 400000;
constexpr int NB = 256;
constexpr int NPB = 49;
constexpr int NP = 256;
constexpr int CH = 1564;
constexpr int CAP = 96;
constexpr unsigned OOB = 0x80000000u;

constexpr size_t WS_TAB = 0;
constexpr size_t WS_ENT = WS_TAB + (size_t)NP * NB * 4;
constexpr size_t WS_WQF = WS_ENT + (size_t)NP * CH * 4 + 256;
constexpr size_t WS_WVF = WS_WQF + 32768;

#define WARM1 10
#define WARM2 16
constexpr int SLICE_LINES = 784;

#define STAMP(buf, slot)
#define STAMP_ARG

__device__ __forceinline__ unsigned pk_bf16(float a, float b) {
    bf16x2 v = {(__bf16)a, (__bf16)b};
    return __builtin_bit_cast(unsigned, v);
}
template <int CTRL, int ROWM = 0xf, int BANKM = 0xf>
__device__ __forceinline__ float dpp_f(float old, float v) {
    return __builtin_bit_cast(float, __builtin_amdgcn_update_dpp(__builtin_bit_cast(int, old), __builtin_bit_cast(int, v), CTRL, ROWM, BANKM, false));
}
template <int CTRL, int ROWM = 0xf>
__device__ __forceinline__ int dpp_add_i(int v) {
    return v + __builtin_amdgcn_update_dpp(0, v, CTRL, ROWM, 0xf, false);
}
__device__ __forceinline__ int wave_incl_scan(int v) {
    v = dpp_add_i<0x111>(v);
    v = dpp_add_i<0x112>(v);
    v = dpp_add_i<0x114>(v);
    v = dpp_add_i<0x118>(v);
    v = dpp_add_i<0x142, 0xa>(v);
    v = dpp_add_i<0x143, 0xc>(v);
    return v;
}
__device__ __forceinline__ float dot2(unsigned a, unsigned b, float c) {
    return __builtin_amdgcn_fdot2_f32_bf16(__builtin_bit_cast(bf16x2, a), __builtin_bit_cast(bf16x2, b), c, false);
}
__device__ __forceinline__ float dot8(u32x4 a, u32x4 b, float c) {
    return dot2(a.w, b.w, dot2(a.z, b.z, dot2(a.y, b.y, dot2(a.x, b.x, c))));
}
__device__ __forceinline__ f32x4 mfma16(u32x4 a, u32x4 b, f32x4 c) {
    return __builtin_amdgcn_mfma_f32_16x16x32_bf16(__builtin_bit_cast(bf16x8, a), __builtin_bit_cast(bf16x8, b), c, 0, 0, 0);
}
__device__ __forceinline__ float sigmoid_logit(float s) {
    const float k = -0.08838834764831845f * 1.4426950408889634f;
    return __builtin_amdgcn_rcpf(1.0f + __builtin_amdgcn_exp2f(s * k));
}

__global__ __launch_bounds__(512) void k1_partition(const int* __restrict__ src, const int* __restrict__ dst,
                                                   const unsigned short* __restrict__ wq, const unsigned short* __restrict__ wv,
                                                   const void* __restrict__ x, char* __restrict__ ws STAMP_ARG) {
    __shared__ __attribute__((aligned(16))) unsigned hist[NB];
    __shared__ __attribute__((aligned(16))) unsigned offs[NB];
    __shared__ __attribute__((aligned(16))) unsigned sorted[CH + 4];
    STAMP(stamps, 0);
    const int t = threadIdx.x, w = blockIdx.x;
    const int e0 = w * CH + t * 4;
    const bool act = t < CH / 4 && e0 < EE;
    u32x4 d4 = {0, 0, 0, 0}, s4 = {0, 0, 0, 0};
    if (act) {
        d4 = *(const u32x4*)(dst + e0);
        s4 = *(const u32x4*)(src + e0);
    }
    if (t < NB) hist[t] = 0;
    unsigned warm = 0;
    {
        auto rx = __builtin_amdgcn_make_buffer_rsrc((void*)x, 0, NN * 256, 0x00020000);
        constexpr int L1 = SLICE_LINES * WARM1 / 16;
#pragma unroll
        for (int o = 0; o < L1; o += 512)
            if (o + t < L1) warm ^= __builtin_amdgcn_raw_buffer_load_b32(rx, ((w >> 3) * SLICE_LINES + o + t) * 128, 0, 0);
    }
    if (t >= 496) {
        const int id = w * 16 + (t - 496), mat = id >> 11, nt = (id >> 8) & 7, s = (id >> 6) & 3, g = (id >> 4) & 3, m = id & 15;
        const unsigned short* wm = mat ? wv : wq;
        unsigned short h[8];
#pragma unroll
        for (int j = 0; j < 8; j++) {
            const int k = mat ? 64 * (s >> 1) + 8 * j + 4 * (g & 1) + 2 * (s & 1) + (g >> 1) : 32 * s + 8 * g + j;
            h[j] = wm[k * 128 + 16 * nt + m];
        }
        u32x4 f = {h[0] | (unsigned)h[1] << 16, h[2] | (unsigned)h[3] << 16, h[4] | (unsigned)h[5] << 16, h[6] | (unsigned)h[7] << 16};
        *(u32x4*)(ws + WS_WQF + (size_t)id * 16) = f;
    }
    __syncthreads();
    STAMP(stamps, 1);
    unsigned b[4], r[4], ent[4];
    if (act) {
#pragma unroll
        for (int j = 0; j < 4; j++) {
            const unsigned d = d4[j];
            b[j] = min(d / NPB, (unsigned)(NB - 1));
            ent[j] = (s4[j] & 0xffffu) | (d - b[j] * NPB) << 16;
            r[j] = atomicAdd(&hist[b[j]], 1u);
        }
    }
    __syncthreads();
    STAMP(stamps, 2);
    if (t < 64) {
        const u32x4 c = *(const u32x4*)&hist[t * 4];
        const int tot = c.x + c.y + c.z + c.w;
        const unsigned ex = wave_incl_scan(tot) - tot;
        const u32x4 o = {ex, ex + c.x, ex + c.x + c.y, ex + c.x + c.y + c.z};
        *(u32x4*)&offs[t * 4] = o;
        *(u32x4*)(ws + WS_TAB + ((size_t)w * NB + t * 4) * 4) = o | c << 16;
    }
    __syncthreads();
    if (act) {
#pragma unroll
        for (int j = 0; j < 4; j++) sorted[offs[b[j]] + r[j]] = ent[j];
    }
    __syncthreads();
    STAMP(stamps, 3);
    if (act) *(u32x4*)(ws + WS_ENT + (size_t)e0 * 4) = *(const u32x4*)&sorted[t * 4];
    asm volatile("" ::"v"(warm));
    STAMP(stamps, 4);
}

constexpr int QP = 272;
constexpr int AP = 528;
constexpr int L_LIST = 0;
constexpr int L_SENT = NPB * CAP * 4;
constexpr int L_CNT = L_SENT + 64;
constexpr int L_XS = L_CNT + 256;
constexpr int L_QS = L_XS + 64 * QP;
constexpr int L_AGG = L_QS + 64 * QP;
constexpr int L_WV = L_AGG + 64 * AP;
constexpr int L_END = L_WV + 32768;

struct Walk { int n, i, ng, left; };

__global__ __launch_bounds__(1024) void k2_aggregate(const void* __restrict__ x, const char* __restrict__ ws,
                                                    unsigned short* __restrict__ out STAMP_ARG) {
    __shared__ __attribute__((aligned(16))) char lds[L_END];
    STAMP(stamps, 0);
    const int t = threadIdx.x, lane = t & 63, v = __builtin_amdgcn_readfirstlane(t >> 6);
    const int b = (blockIdx.x & 7) * 32 + (blockIdx.x >> 3);
    auto rx = __builtin_amdgcn_make_buffer_rsrc((void*)x, 0, NN * 256, 0x00020000);
    unsigned* cnt = (unsigned*)(lds + L_CNT);

    const int pw = t >> 2;
    const unsigned tv = ((const unsigned*)(ws + WS_TAB))[pw * NB + b];
    const u32x4 xo = __builtin_amdgcn_raw_buffer_load_b128(rx, t < NPB * 16 ? b * NPB * 256 + t * 16 : OOB, 0, 0);
    const int nt = v & 7, mh = v >> 3;
    u32x4 wf[4];
#pragma unroll
    for (int s = 0; s < 4; s++) wf[s] = *(const u32x4*)(ws + WS_WQF + ((nt * 4 + s) * 64 + lane) * 16);
    unsigned warm = 0;
    {
        constexpr int L0 = SLICE_LINES * WARM1 / 16, L1 = SLICE_LINES * WARM2 / 16;
        if (L1 > L0 && t < L1 - L0) warm = __builtin_amdgcn_raw_buffer_load_b32(rx, ((blockIdx.x >> 3) * SLICE_LINES + L0 + t) * 128, 0, 0);
    }
    {
        const u32x4 so = {OOB, OOB, OOB, OOB}, z = {0, 0, 0, 0};
        for (int o = t * 16; o < L_CNT; o += 16384) *(u32x4*)(lds + o) = so;
        if (t < 16) *(u32x4*)(lds + L_CNT + t * 16) = z;
        for (int o = t * 16; o < 64 * AP; o += 16384) *(u32x4*)(lds + L_AGG + o) = z;
    }
    *(u32x4*)(lds + L_XS + (t >> 4) * QP + (t & 15) * 16) = xo;
    const int seg_n = tv >> 16;
    const unsigned* seg = (const unsigned*)(ws + WS_ENT) + pw * CH + (tv & 0xffffu);
    int ei = (t & 3) * 4;
    u32x4 e4 = {0, 0, 0, 0};
    if (ei < seg_n) e4 = u32x4{seg[ei], seg[ei + 1], seg[ei + 2], seg[ei + 3]};
    __syncthreads();
    STAMP(stamps, 1);
    {
        const int g = lane >> 4, m = lane & 15;
#pragma unroll
        for (int k = 0; k < 2; k++) {
            const int row = (mh * 2 + k) * 16 + m;
            f32x4 d = {0, 0, 0, 0};
#pragma unroll
            for (int s = 0; s < 4; s++) d = mfma16(wf[s], *(const u32x4*)(lds + L_XS + row * QP + s * 64 + g * 16), d);
            *(u32x2*)(lds + L_QS + row * QP + nt * 32 + g * 8) = u32x2{pk_bf16(d[0], d[1]), pk_bf16(d[2], d[3])};
        }
    }
    const u32x4 wv0 = *(const u32x4*)(ws + WS_WVF + t * 16), wv1 = *(const u32x4*)(ws + WS_WVF + 16384 + t * 16);
    while (true) {
#pragma unroll
        for (int j = 0; j < 4; j++) {
            if (ei + j < seg_n) {
                const unsigned n = min(e4[j] >> 16, (unsigned)(NPB - 1));
                const unsigned r = atomicAdd(&cnt[n], 1u);
                if (r < CAP) *(unsigned*)(lds + L_LIST + (n * CAP + r) * 4) = (e4[j] & 0xffffu) << 8;
                else cnt[63] = 1;
            }
        }
        ei += 16;
        if (ei >= seg_n) break;
        e4 = u32x4{seg[ei], seg[ei + 1], seg[ei + 2], seg[ei + 3]};
    }
    __syncthreads();
    STAMP(stamps, 2);
    *(u32x4*)(lds + L_WV + t * 16) = wv0;
    *(u32x4*)(lds + L_WV + 16384 + t * 16) = wv1;

    if (__builtin_amdgcn_readfirstlane(cnt[63]) == 0) {
        const int ngv = lane < NPB ? (cnt[lane] + 7) >> 3 : 0;
        const int incl = wave_incl_scan(ngv);
        const int total = __builtin_amdgcn_readlane(incl, 63);
        const int nbat = (total + 3) >> 2;
        const int b0 = (nbat * v) >> 4, b1 = (nbat * (v + 1)) >> 4;
        const unsigned long long nz = __ballot(ngv > 0);
        if (b1 > b0) {
            Walk wk;
            wk.n = __popcll(__ballot(lane < NPB && incl <= 4 * b0));
            wk.ng = __builtin_amdgcn_readlane(ngv, wk.n);
            wk.i = 4 * b0 - (__builtin_amdgcn_readlane(incl, wk.n) - wk.ng);
            wk.left = min(4 * b1, total) - 4 * b0;
            const int c16 = (lane & 7) * 16, p4 = (lane >> 3) * 4;
            const int jj = lane & 7;
            unsigned am[4];
#pragma unroll
            for (int r = 0; r < 4; r++) am[r] = (jj >> 1) == r ? ((jj & 1) ? 0xffff0000u : 0x0000ffffu) : 0u;
            const bool b0s = lane & 1, b1s = lane & 2;
            const bool fl_valid = (lane >> 5) == ((lane >> 3) & 1);
            const int fl_off = L_AGG + ((lane >> 4) & 1) * 32 + (lane & 7) * 4;
            f32x4 acc0 = {0, 0, 0, 0}, acc1 = {0, 0, 0, 0};

            auto load_batch = [&](u32x4 (&X)[8], int (&nd)[4], int (&fl)[4]) {
                unsigned off[4];
#pragma unroll
                for (int k = 0; k < 4; k++) {
                    const bool live = wk.left > 0;
                    const int la = live ? L_LIST + (wk.n * CAP + wk.i * 8) * 4 : L_SENT;
                    off[k] = *(const unsigned*)(lds + la + p4);
                    nd[k] = wk.n;
                    fl[k] = live && (wk.i + 1 == wk.ng || wk.left == 1);
                    if (live) {
                        wk.left--;
                        wk.i++;
                        if (wk.i == wk.ng && wk.left > 0) {
                            wk.n += 1 + __builtin_ctzll(nz >> (wk.n + 1));
                            wk.i = 0;
                            wk.ng = __builtin_amdgcn_readlane(ngv, wk.n);
                        }
                    }
                }
#pragma unroll
                for (int k = 0; k < 4; k++) {
                    X[2 * k] = __builtin_amdgcn_raw_buffer_load_b128(rx, off[k] | c16, 0, 0);
                    X[2 * k + 1] = __builtin_amdgcn_raw_buffer_load_b128(rx, (off[k] | c16) + 128, 0, 0);
                }
            };
            auto compute_batch = [&](const u32x4 (&X)[8], const int (&nd)[4], const int (&fl)[4]) {
                float p[4];
#pragma unroll
                for (int k = 0; k < 4; k++) {
                    const char* q = lds + L_QS + nd[k] * QP + c16;
                    p[k] = dot8(X[2 * k + 1], *(const u32x4*)(q + 128), dot8(X[2 * k], *(const u32x4*)q, 0.f));
                }
                const float r01 = (b0s ? p[1] : p[0]) + dpp_f<0xB1>(0.f, b0s ? p[0] : p[1]);
                const float r23 = (b0s ? p[3] : p[2]) + dpp_f<0xB1>(0.f, b0s ? p[2] : p[3]);
                const float r = (b1s ? r23 : r01) + dpp_f<0x4E>(0.f, b1s ? r01 : r23);
                const float o4 = dpp_f<0x114, 0xf, 0xa>(dpp_f<0x104, 0xf, 0x5>(0.f, r), r);
                const float al = sigmoid_logit(r + o4);
                const unsigned ap = pk_bf16(al, al);
#define GROUP(k)                                                                                       \
                {                                                                                      \
                    const unsigned a = __builtin_amdgcn_update_dpp(0, (int)ap, (k) * 0x55, 0xf, 0xf, false); \
                    const u32x4 A = {a & am[0], a & am[1], a & am[2], a & am[3]};                      \
                    acc0 = mfma16(A, X[2 * (k)], acc0);                                                \
                    acc1 = mfma16(A, X[2 * (k) + 1], acc1);                                            \
                    if (fl[k]) {                                                                       \
                        if (fl_valid) {                                                                \
                            float* ag = (float*)(lds + fl_off + nd[k] * AP);                           \
                            _Pragma("unroll") for (int rr = 0; rr < 4; rr++) {                         \
                                atomicAdd(ag + rr * 16, acc0[rr]);                                     \
                                atomicAdd(ag + 64 + rr * 16, acc1[rr]);                                \
                            }                                                                          \
                        }                                                                              \
                        acc0 = f32x4{0, 0, 0, 0};                                                      \
                        acc1 = f32x4{0, 0, 0, 0};                                                      \
                    }                                                                                  \
                }
                GROUP(0) GROUP(1) GROUP(2) GROUP(3)
#undef GROUP
            };
            u32x4 XA[8], XB[8];
            int nA[4], fA[4], nB[4], fB[4];
            int nleft = b1 - b0;
            load_batch(XA, nA, fA);
            while (true) {
                if (nleft == 1) { compute_batch(XA, nA, fA); break; }
                load_batch(XB, nB, fB);
                compute_batch(XA, nA, fA);
                if (nleft == 2) { compute_batch(XB, nB, fB); break; }
                load_batch(XA, nA, fA);
                compute_batch(XB, nB, fB);
                nleft -= 2;
            }
        }
    } else {
        const int f = 2 * lane, ia = 64 * (f >> 6) + 16 * (f & 3) + 8 * ((f >> 2) & 1) + ((f >> 3) & 7);
        for (int w = v; w < NP; w += 16) {
            const unsigned tw = ((const unsigned*)(ws + WS_TAB))[w * NB + b];
            const unsigned* sg = (const unsigned*)(ws + WS_ENT) + w * CH + (tw & 0xffffu);
            for (int i = 0; i < (int)(tw >> 16); i++) {
                const unsigned e = sg[i], n = min(e >> 16, (unsigned)(NPB - 1));
                const unsigned xv = __builtin_amdgcn_raw_buffer_load_b32(rx, (e & 0xffffu) * 256 + lane * 4, 0, 0);
                float s = dot2(xv, *(const unsigned*)(lds + L_QS + n * QP + lane * 4), 0.f);
#pragma unroll
                for (int o = 32; o > 0; o >>= 1) s += __shfl_xor(s, o, 64);
                const float al = (float)(__bf16)sigmoid_logit(s);
                float* ag = (float*)(lds + L_AGG + n * AP) + ia;
                atomicAdd(ag, al * __builtin_bit_cast(float, xv << 16));
                atomicAdd(ag + 16, al * __builtin_bit_cast(float, xv & 0xffff0000u));
            }
        }
    }
    STAMP(stamps, 3);
    __syncthreads();
    STAMP(stamps, 4);
    {
        const int g = lane >> 4, m = lane & 15;
#pragma unroll
        for (int s = 0; s < 4; s++) wf[s] = *(const u32x4*)(lds + L_WV + ((nt * 4 + s) * 64 + lane) * 16);
#pragma unroll
        for (int k = 0; k < 2; k++) {
            const int row = (mh * 2 + k) * 16 + m;
            f32x4 d = {0, 0, 0, 0};
#pragma unroll
            for (int s = 0; s < 4; s++) {
                const f32x4 a0 = *(const f32x4*)(lds + L_AGG + row * AP + s * 128 + g * 32);
                const f32x4 a1 = *(const f32x4*)(lds + L_AGG + row * AP + s * 128 + g * 32 + 16);
                d = mfma16(wf[s], u32x4{pk_bf16(a0[0], a0[1]), pk_bf16(a0[2], a0[3]), pk_bf16(a1[0], a1[1]), pk_bf16(a1[2], a1[3])}, d);
            }
            *(u32x2*)(lds + L_XS + row * QP + nt * 32 + g * 8) = u32x2{pk_bf16(d[0], d[1]), pk_bf16(d[2], d[3])};
        }
    }
    __syncthreads();
    if (t < NPB * 16 && b * NPB + (t >> 4) < NN)
        *(u32x4*)((char*)out + (size_t)b * NPB * 256 + t * 16) = *(const u32x4*)(lds + L_XS + (t >> 4) * QP + (t & 15) * 16);
    asm volatile("" ::"v"(warm));
    STAMP(stamps, 5);
}

#define ST1
#define ST2

extern "C" void kernel_launch(void* const* d_in, const int* in_sizes, int n_in, void* d_out, int out_size,
                              void* d_ws, size_t ws_size, hipStream_t stream) {
    const void* x = d_in[0];
    const int* src = (const int*)d_in[1];
    const int* dst = (const int*)d_in[2];
    const unsigned short* wq = (const unsigned short*)d_in[3];
    const unsigned short* wv = (const unsigned short*)d_in[4];
    k1_partition<<<NP, 512, 0, stream>>>(src, dst, wq, wv, x, (char*)d_ws ST1);
    k2_aggregate<<<NB, 1024, 0, stream>>>(x, (const char*)d_ws, (unsigned short*)d_out ST2);
}
```

```cpp
#include <hip/hip_runtime.h>
#include <hip/hip_bf16.h>

typedef unsigned u32x4 __attribute__((ext_vector_type(4)));
typedef unsigned u32x2 __attribute__((ext_vector_type(2)));
typedef int i32x4 __attribute__((ext_vector_type(4)));
typedef float f32x4 __attribute__((ext_vector_type(4)));
typedef __bf16 bf16x8 __attribute__((ext_vector_type(8)));
typedef __bf16 bf16x2 __attribute__((ext_vector_type(2)));

constexpr int NN = 12500, EE = 400000;
constexpr int NB = 256;
constexpr int NPB = 49;
constexpr int NP = 256;
constexpr int CH = 1564;
constexpr int CAPW = 192;
constexpr unsigned OOB = 0x80000000u;
constexpr unsigned SENT = 0x00ffffffu;

constexpr unsigned WS_TAB = 0;
constexpr unsigned WS_ENT = WS_TAB + NP * NB * 4;
constexpr unsigned WS_END = WS_ENT + NP * CH * 4 + 256;

#define WARM1 8
constexpr int SLICE_LINES = 784;

#define STAMP(buf, slot)
#define STAMP_ARG

__device__ __forceinline__ unsigned pk_bf16(float a, float b) {
    bf16x2 v = {(__bf16)a, (__bf16)b};
    return __builtin_bit_cast(unsigned, v);
}
template <int CTRL, int ROWM = 0xf>
__device__ __forceinline__ int dpp_add_i(int v) {
    return v + __builtin_amdgcn_update_dpp(0, v, CTRL, ROWM, 0xf, false);
}
__device__ __forceinline__ int wave_incl_scan(int v) {
    v = dpp_add_i<0x111>(v);
    v = dpp_add_i<0x112>(v);
    v = dpp_add_i<0x114>(v);
    v = dpp_add_i<0x118>(v);
    v = dpp_add_i<0x142, 0xa>(v);
    v = dpp_add_i<0x143, 0xc>(v);
    return v;
}
__device__ __forceinline__ f32x4 mfma16(u32x4 a, u32x4 b, f32x4 c) {
    return __builtin_amdgcn_mfma_f32_16x16x32_bf16(__builtin_bit_cast(bf16x8, a), __builtin_bit_cast(bf16x8, b), c, 0, 0, 0);
}
__device__ __forceinline__ i32x4 make_srd(const void* p, unsigned bytes) {
    const unsigned long long a = (unsigned long long)p;
    return i32x4{(int)a, (int)(a >> 32) & 0xffff, (int)bytes, 0x00020000};
}

__global__ __launch_bounds__(448) void k1_partition(const int* __restrict__ src, const int* __restrict__ dst,
                                                   const void* __restrict__ x, char* __restrict__ ws STAMP_ARG) {
    __shared__ __attribute__((aligned(16))) unsigned hist[NB];
    __shared__ __attribute__((aligned(16))) unsigned offs[NB];
    __shared__ __attribute__((aligned(16))) unsigned sorted[CH + 4];
    STAMP(stamps, 0);
    const int t = threadIdx.x, w = blockIdx.x;
    if (w >= NP) {
        const int hw = w - NP;
        auto rx = __builtin_amdgcn_make_buffer_rsrc((void*)x, 0, NN * 256, 0x00020000);
        constexpr int L1 = SLICE_LINES * WARM1 / 16;
        unsigned warm = 0;
#pragma unroll
        for (int o = 0; o < L1; o += 448)
            warm ^= __builtin_amdgcn_raw_buffer_load_b32(rx, o + t < L1 ? ((hw >> 3) * SLICE_LINES + o + t) * 128 : OOB, 0, 0);
        asm volatile("" ::"v"(warm));
        STAMP(stamps, 5);
        return;
    }
    const int e0 = w * CH + t * 4;
    const bool act = t < CH / 4 && e0 < EE;
    u32x4 d4 = {0, 0, 0, 0}, s4 = {0, 0, 0, 0};
    if (act) {
        d4 = *(const u32x4*)(dst + e0);
        s4 = *(const u32x4*)(src + e0);
    }
    if (t < NB) hist[t] = 0;
    __syncthreads();
    STAMP(stamps, 1);
    unsigned b[4], r[4], ent[4];
    if (act) {
#pragma unroll
        for (int j = 0; j < 4; j++) {
            const unsigned d = d4[j];
            b[j] = min((d * 21400u) >> 20, (unsigned)(NB - 1));
            r[j] = atomicAdd(&hist[b[j]], 1u);
            const unsigned dl = d - b[j] * NPB;
            ent[j] = dl << 28 | (s4[j] & 0xffffu) << 8 | (dl >> 4 & 3);
        }
    }
    __syncthreads();
    STAMP(stamps, 2);
    {
        const int l = t & 63;
        const u32x4 c = *(const u32x4*)&hist[l * 4];
        const int tot = c.x + c.y + c.z + c.w;
        const unsigned ex = wave_incl_scan(tot) - tot;
        const u32x4 o = {ex, ex + c.x, ex + c.x + c.y, ex + c.x + c.y + c.z};
        *(volatile u32x4*)&offs[l * 4] = o;
        if (t >= 384) *(u32x4*)(ws + WS_TAB + (w * NB + l * 4) * 4) = o | c << 16;
    }
    __builtin_amdgcn_wave_barrier();
    if (act) {
#pragma unroll
        for (int j = 0; j < 4; j++) sorted[((volatile unsigned*)offs)[b[j]] + r[j]] = ent[j];
    }
    __syncthreads();
    STAMP(stamps, 3);
    if (act) *(u32x4*)(ws + WS_ENT + e0 * 4) = *(const u32x4*)&sorted[t * 4];
    STAMP(stamps, 4);
}

constexpr int QP = 272, QT = 16 * QP + 16;
constexpr int L_CNT = 0;
constexpr int L_LIST = 64;
constexpr int L_QS = L_LIST + 16 * CAPW * 4;
constexpr int L_X = (L_QS + 4 * QT + 255) / 256 * 256;
constexpr int L_WQ = L_X + 32768;
constexpr int L_OUT = L_X + 4096;
constexpr int L_END = L_X + 16 * 8192;

#define PRIO(r) "s_setprio 0\n s_cmp_lt_i32 " r ", 1\n s_cbranch_scc1 8f\n s_setprio 1\n s_cmp_lt_i32 " r ", 2\n s_cbranch_scc1 8f\n" \
                "s_setprio 2\n s_cmp_lt_i32 " r ", 3\n s_cbranch_scc1 8f\n s_setprio 3\n 8:\n"
#define STR_(x) #x
#define STR(x) STR_(x)
#define DMA_BLK(ent, srd) \
    "buffer_load_dwordx4 " ent ", " srd ", 0 offen lds\n" \
    "s_add_u32 m0, m0, 0x400\n s_nop 0\n" \
    "buffer_load_dwordx4 " ent ", " srd ", %[c128] offen lds\n" \
    "s_add_u32 m0, m0, 0x400\n"
#define SRD_(i) "ds_read_b128 v[32+4*" #i ":35+4*" #i "], %[s0] offset:512*(" #i "/2)+4096*(" #i "&1)\n"
#define TRD_(i, a) "ds_read_b64_tr_b16 v[32+4*" #i ":33+4*" #i "], %[" a "] offset:512*(" #i "/2)\n" \
                   "ds_read_b64_tr_b16 v[34+4*" #i ":35+4*" #i "], %[" a "] offset:512*(" #i "/2)+4096\n"
#define SMF_(i, n, d, c) "s_waitcnt lgkmcnt(" #n ")\n v_mfma_f32_16x16x32_bf16 " d ", v[32+4*" #i ":35+4*" #i "], v[96+4*(" #i "/2):99+4*(" #i "/2)], " c "\n"
#define PMF_(i) "v_mfma_f32_16x16x32_bf16 v[64+4*" #i ":67+4*" #i "], v[32+4*" #i ":35+4*" #i "], v[120:123], v[64+4*" #i ":67+4*" #i "]\n"
#define FL_(i) "v_cvt_pk_bf16_f32 v[32+2*" #i "], v[64+4*" #i "], v[65+4*" #i "]\n v_cvt_pk_bf16_f32 v[33+2*" #i "], v[66+4*" #i "], v[67+4*" #i "]\n" \
               "ds_write_b64 %[fa], v[32+2*" #i ":33+2*" #i "] offset:32*" #i "\n"
#define WTR_(s, a, o, h) "ds_read_b64_tr_b16 v[96+4*" #s ":97+4*" #s "], %[" a "] offset:" #o "\n" \
                         "ds_read_b64_tr_b16 v[98+4*" #s ":99+4*" #s "], %[" a "] offset:" #o "+" #h "\n"
#define BRD_(s, ks, km) "ds_read_b128 v[32+8*" #s ":35+8*" #s "], %[gb] offset:" #ks "*" #s "\n" \
                        "ds_read_b128 v[36+8*" #s ":39+8*" #s "], %[gb] offset:" #ks "*" #s "+" #km "\n"
#define GMF_(s, n, c0, c1) "s_waitcnt lgkmcnt(" #n ")\n" \
    "v_mfma_f32_16x16x32_bf16 v[112:115], v[96+4*" #s ":99+4*" #s "], v[32+8*" #s ":35+8*" #s "], " c0 "\n" \
    "v_mfma_f32_16x16x32_bf16 v[116:119], v[96+4*" #s ":99+4*" #s "], v[36+8*" #s ":39+8*" #s "], " c1 "\n"
#define GEMM_MF GMF_(0, 12, "0", "0") GMF_(1, 8, "v[112:115]", "v[116:119]") GMF_(2, 4, "v[112:115]", "v[116:119]") GMF_(3, 0, "v[112:115]", "v[116:119]")
#define V10(a) "v" STR(a) "0", "v" STR(a) "1", "v" STR(a) "2", "v" STR(a) "3", "v" STR(a) "4", "v" STR(a) "5", "v" STR(a) "6", "v" STR(a) "7", "v" STR(a) "8", "v" STR(a) "9"
#define CLOB_V "v25", "v26", "v27", "v28", "v29", V10(3), V10(4), V10(5), V10(6), V10(7), V10(8), V10(9), V10(10), V10(11), "v120", "v121", "v122", "v123"
#define CLOB_S "s40", "s41", "s42", "s43", "s44", "s45", "s46", "s47", "vcc", "scc", "memory"
#define AST(k)
#define CLOB_ST
#define TSTM(x)
#define TST_INIT
#define TST_ARG
#define TST_CLOB

__global__ __launch_bounds__(1024) void k2_aggregate(const void* __restrict__ x, const void* __restrict__ wq, const void* __restrict__ wv,
                                                    const char* __restrict__ ws, char* __restrict__ out STAMP_ARG) {
    __shared__ __attribute__((aligned(256))) char lds[L_END];
    int t = threadIdx.x;
    const int v = __builtin_amdgcn_readfirstlane(t >> 6);
    const int b = (blockIdx.x & 7) * 32 + (blockIdx.x >> 3);
    const i32x4 sx = make_srd(x, NN * 256), sw = make_srd(ws, WS_END), sq = make_srd(wq, 32768), sv = make_srd(wv, 32768);
    const unsigned d0 = 64 * ((t >> 5) & 1) + 16 * ((t & 3) ^ ((t >> 3) & 2));
    const unsigned wa = (32 * (v >> 2) + 16 * (v & 1) + 4 * ((v >> 1) & 1) + 8 * ((t >> 4) & 1) + ((t >> 2) & 3)) * 256 + d0;
    unsigned tv;
    {
        const unsigned ta = WS_TAB + ((t >> 2) * NB + b) * 4;
        const unsigned xa = (b * NPB + 8 * (v >> 1) + ((t >> 2) & 7)) * 256 + d0 + 128 * (v & 1);
        asm volatile(
            AST(0)
            "buffer_load_dword v24, %[ta], %[rw], 0 offen\n"
            "s_mov_b32 m0, %[mx]\n s_nop 0\n"
            "buffer_load_dwordx4 %[xa], %[rx], 0 offen lds\n"
            "s_mov_b32 m0, %[mw]\n s_nop 0\n"
            DMA_BLK("%[wa]", "%[rq]")
            : "={v24}"(tv)
            : [ta] "v"(ta), [xa] "v"(xa), [wa] "v"(wa), [rw] "s"(sw), [rx] "s"(sx), [rq] "s"(sq), [c128] "s"(128),
              [mx] "s"(L_X + 1024 * v), [mw] "s"(L_WQ + 2048 * v)
            : "scc", "memory" CLOB_ST);
    }
    asm volatile("" : "+v"(t));
    const int lane = t & 63, g = lane >> 4, m = t & 15, nt = v & 7, mh = v >> 3;
    if (t < 16 * CAPW / 4) *(u32x4*)(lds + L_LIST + t * 16) = u32x4{SENT, SENT, SENT, SENT};
    else if (t < 16 * CAPW / 4 + 16) *(unsigned*)(lds + L_CNT + m * 4) = L_LIST + m * CAPW * 4;
    const unsigned lbase = L_LIST + v * CAPW * 4;
    const unsigned buf = L_X + v * 8192;
    const unsigned trl = 256 * (g & 1) + 64 * (m >> 2) + 16 * (((t >> 1) & 1) ^ (2 * (g & 1))) + 8 * (t & 1);
    const unsigned trn = (trl ^ (32 * (nt & 1))) + 512 * (nt >> 1);
    const unsigned srl = 2048 * (m >> 3) + 64 * (m & 7) + 16 * (g ^ ((m >> 1) & 2));
    const unsigned prow = 2 * mh * QT + m * QP;
    const unsigned lp = lbase + ((t >> 2) & 7) * 4;
    unsigned cnt_l, cnt_v;
    {
        const unsigned ei = (t & 3) * 4;
        const unsigned eb = WS_ENT + ((t >> 2) * CH + ei) * 4;
        const unsigned gb = L_X + 8192 * mh + srl;
        asm volatile(
            "v_bfrev_b32 v27, 1\n"
            "s_waitcnt vmcnt(3)\n"
            AST(1)
            "v_and_b32 v25, 0xffff, v24\n"
            "v_sub_u32_sdwa v24, v24, %[ei] dst_sel:DWORD dst_unused:UNUSED_PAD src0_sel:WORD_1 src1_sel:DWORD\n"
            "v_lshl_add_u32 v25, v25, 2, %[eb]\n"
            "v_cmp_lt_i32_e64 s[40:41], 0, v24\n v_cmp_lt_i32_e64 s[42:43], 1, v24\n"
            "v_cmp_lt_i32_e64 s[44:45], 2, v24\n v_cmp_lt_i32_e64 s[46:47], 3, v24\n"
            "v_cndmask_b32_e64 v27, v27, v25, s[40:41]\n"
            "buffer_load_dwordx4 v[28:31], v27, %[rw], 0 offen\n"
            "s_waitcnt vmcnt(1) lgkmcnt(0)\n"
            "s_barrier\n"
            AST(2)
            WTR_(0, "tq", 0, 4096) BRD_(0, 512, 4096) WTR_(1, "tq", 8192, 4096) BRD_(1, 512, 4096)
            WTR_(2, "tq", 16384, 4096) BRD_(2, 512, 4096) WTR_(3, "tq", 24576, 4096) BRD_(3, 512, 4096)
            GEMM_MF
            "v_mov_b32 v26, 4\n"
            "s_nop 5\n"
            "v_cvt_pk_bf16_f32 v112, v112, v113\n v_cvt_pk_bf16_f32 v113, v114, v115\n"
            "v_cvt_pk_bf16_f32 v116, v116, v117\n v_cvt_pk_bf16_f32 v117, v118, v119\n"
            "ds_write_b64 %[qw], v[112:113]\n ds_write_b64 %[qw], v[116:117] offset:4368\n"
            AST(3)
            "5:\n"
            "s_waitcnt vmcnt(0)\n"
            "v_lshrrev_b32 v120, 26, v28\n v_lshrrev_b32 v121, 26, v29\n v_lshrrev_b32 v122, 26, v30\n v_lshrrev_b32 v123, 26, v31\n"
            "s_mov_b64 exec, s[40:41]\n ds_add_rtn_u32 v120, v120, v26\n"
            "s_mov_b64 exec, s[42:43]\n ds_add_rtn_u32 v121, v121, v26\n"
            "s_mov_b64 exec, s[44:45]\n ds_add_rtn_u32 v122, v122, v26\n"
            "s_mov_b64 exec, s[46:47]\n ds_add_rtn_u32 v123, v123, v26\n"
            "s_mov_b64 exec, -1\n"
            "v_cmp_lt_i32 vcc, 16, v24\n"
            "s_waitcnt lgkmcnt(0)\n"
            "v_min_u32 v120, %[lmax], v120\n v_min_u32 v121, %[lmax], v121\n v_min_u32 v122, %[lmax], v122\n v_min_u32 v123, %[lmax], v123\n"
            "s_mov_b64 exec, s[40:41]\n ds_write_b32 v120, v28\n"
            "s_mov_b64 exec, s[42:43]\n ds_write_b32 v121, v29\n"
            "s_mov_b64 exec, s[44:45]\n ds_write_b32 v122, v30\n"
            "s_mov_b64 exec, s[46:47]\n ds_write_b32 v123, v31\n"
            "s_mov_b64 exec, -1\n"
            "s_cbranch_vccnz 6f\n"
            "s_waitcnt lgkmcnt(0)\n"
            AST(4)
            "s_barrier\n"
            "ds_read_b32 v24, %[ca]\n"
            "ds_read2_b32 v[28:29], %[lp] offset0:0 offset1:8\n"
            "ds_read2_b32 v[30:31], %[lp] offset0:16 offset1:24\n"
            "s_mov_b32 m0, %[buf]\n"
            "s_waitcnt lgkmcnt(2)\n"
            "v_readlane_b32 %[cnt], v24, %[wv]\n"
            "s_waitcnt lgkmcnt(0)\n"
            "v_and_or_b32 v28, v28, %[msk], %[d0]\n v_and_or_b32 v29, v29, %[msk], %[d0]\n"
            "v_and_or_b32 v30, v30, %[msk], %[d0]\n v_and_or_b32 v31, v31, %[msk], %[d0]\n"
            "s_sub_i32 %[cnt], %[cnt], %[lb4]\n"
            "s_max_i32 %[cnt], %[cnt], 0\n"
            "s_lshr_b32 s40, %[cnt], 7\n"
            PRIO("s40")
            DMA_BLK("v28", "%[rx]") "s_nop 0\n" DMA_BLK("v29", "%[rx]") "s_nop 0\n"
            DMA_BLK("v30", "%[rx]") "s_nop 0\n" DMA_BLK("v31", "%[rx]")
            "s_branch 7f\n"
            "6:\n"
            "v_add_u32 v24, -16, v24\n v_add_u32 v25, 64, v25\n v_bfrev_b32 v27, 1\n"
            "v_cmp_lt_i32_e64 s[40:41], 0, v24\n v_cmp_lt_i32_e64 s[42:43], 1, v24\n"
            "v_cmp_lt_i32_e64 s[44:45], 2, v24\n v_cmp_lt_i32_e64 s[46:47], 3, v24\n"
            "s_waitcnt lgkmcnt(0)\n"
            "v_cndmask_b32_e64 v27, v27, v25, s[40:41]\n"
            "buffer_load_dwordx4 v[28:31], v27, %[rw], 0 offen\n"
            "s_branch 5b\n"
            "7:\n"
            AST(5)
            : "+{v24}"(tv), [cnt] "=&s"(cnt_v)
            : [ei] "v"(ei), [eb] "v"(eb), [gb] "v"(gb), [tq] "v"(L_WQ + 2048 * (g >> 1) + trn), [qw] "v"(L_QS + prow + nt * 32 + g * 8),
              [lp] "v"(lp), [ca] "v"(L_CNT + m * 4), [d0] "v"(d0), [wv] "s"(v), [lb4] "s"(lbase + 4),
              [rw] "s"(sw), [rx] "s"(sx), [c128] "s"(128), [msk] "s"(0x00ffff00), [buf] "s"(buf), [lmax] "s"(L_QS - 4)
            : CLOB_V, CLOB_S CLOB_ST);
        cnt_l = tv - (L_LIST + m * CAPW * 4);
        cnt_v = __builtin_amdgcn_readfirstlane(cnt_v);
    }
    const bool generic = __ballot(cnt_l > CAPW * 4) != 0;
    if (!generic) {
        const int a = m >> 2, bq = t & 3;
        const unsigned qrow = L_QS + v * QP + a * QT;
        const unsigned np = lbase + (16 * (bq >> 1) + 4 * g + 2 * (bq & 1)) * 4;
        const unsigned t0 = buf + 2048 * (g >> 1) + trl, t1 = t0 ^ 32;
        unsigned left = cnt_v >> 7, cur = 0, nxt = cnt_v & ~127u, seq = 128, rows = cnt_v & 127;
        asm volatile(
            TST_INIT TSTM(S)
            "ds_read_b128 v[96:99], %[qa]\n ds_read_b128 v[100:103], %[qa] offset:64\n"
            "ds_read_b128 v[104:107], %[qa] offset:128\n ds_read_b128 v[108:111], %[qa] offset:192\n"
            ".irp i,64,65,66,67,68,69,70,71,72,73,74,75,76,77,78,79,80,81,82,83,84,85,86,87,88,89,90,91,92,93,94,95\n v_mov_b32 v\\i, 0\n .endr\n"
            "s_mov_b32 s40, 0xaaaaaaaa\n s_mov_b32 s41, s40\n s_mov_b32 s42, 0xcccccccc\n s_mov_b32 s43, s42\n"
            "s_sub_i32 s44, %[left], 1\n"
            PRIO("s44")
            "1:\n"
            "v_add_u32 v30, %[cur], %[np]\n v_add_u32 v31, %[nxt], %[lp]\n"
            "ds_read_b64 v[28:29], v30\n"
            "ds_read2_b32 v[24:25], v31 offset0:0 offset1:8\n"
            "ds_read2_b32 v[26:27], v31 offset0:16 offset1:24\n"
            "s_mov_b32 %[cur], %[nxt]\n s_mov_b32 %[nxt], %[seq]\n s_add_u32 %[seq], %[seq], 0x80\n"
            "s_waitcnt vmcnt(0)\n"
            TSTM(A)
            SRD_(0) SRD_(1) SRD_(2) SRD_(3) SRD_(4) SRD_(5) SRD_(6) SRD_(7)
            SMF_(0, 7, "v[112:115]", "0") TRD_(0, "t0")
            SMF_(1, 8, "v[116:119]", "0") TRD_(1, "t1")
            SMF_(2, 9, "v[112:115]", "v[112:115]") TRD_(2, "t0")
            SMF_(3, 10, "v[116:119]", "v[116:119]") TRD_(3, "t1")
            SMF_(4, 11, "v[112:115]", "v[112:115]") TRD_(4, "t0")
            SMF_(5, 12, "v[116:119]", "v[116:119]") TRD_(5, "t1")
            SMF_(6, 13, "v[112:115]", "v[112:115]") TRD_(6, "t0")
            SMF_(7, 14, "v[116:119]", "v[116:119]") TRD_(7, "t1")
            "v_and_or_b32 v24, v24, %[msk], %[d0]\n v_and_or_b32 v25, v25, %[msk], %[d0]\n"
            "v_and_or_b32 v26, v26, %[msk], %[d0]\n v_and_or_b32 v27, v27, %[msk], %[d0]\n"
            "v_cmp_eq_u32_sdwa s[44:45], v28, %[av] src0_sel:BYTE_0 src1_sel:DWORD\n"
            "v_cmp_eq_u32_sdwa s[46:47], v29, %[av] src0_sel:BYTE_0 src1_sel:DWORD\n"
            "v_cndmask_b32_e64 v28, v112, v114, s[40:41]\n v_cndmask_b32_e64 v29, v113, v115, s[40:41]\n"
            "v_cndmask_b32_e64 v30, v116, v118, s[40:41]\n v_cndmask_b32_e64 v31, v117, v119, s[40:41]\n"
            "v_cndmask_b32_e64 v28, v28, v30, s[42:43]\n v_cndmask_b32_e64 v29, v29, v31, s[42:43]\n"
            "v_mul_f32 v28, 0xbe0293ee, v28\n v_mul_f32 v29, 0xbe0293ee, v29\n"
            "v_exp_f32 v28, v28\n v_exp_f32 v29, v29\n"
            TSTM(B)
            "s_mov_b32 m0, %[buf]\n"
            "s_cmp_eq_u32 %[left], 0\n"
            "s_waitcnt lgkmcnt(0)\n"
            "s_cbranch_scc1 3f\n"
            DMA_BLK("v24", "%[rx]") "s_cmp_lt_u32 %[rows], 32\n s_cbranch_scc1 9f\n"
            DMA_BLK("v25", "%[rx]") "s_cmp_lt_u32 %[rows], 64\n s_cbranch_scc1 9f\n"
            DMA_BLK("v26", "%[rx]") "s_cmp_lt_u32 %[rows], 96\n s_cbranch_scc1 9f\n"
            DMA_BLK("v27", "%[rx]")
            "9:\n"
            "s_movk_i32 %[rows], 0x7f\n"
            "s_sub_i32 vcc_lo, %[left], 2\n"
            PRIO("vcc_lo")
            "2:\n"
            TSTM(C)
            "v_add_f32 v28, 1.0, v28\n v_add_f32 v29, 1.0, v29\n"
            "v_rcp_f32 v28, v28\n v_rcp_f32 v29, v29\n"
            "s_nop 0\n"
            "v_cndmask_b32_e64 v28, 0, v28, s[44:45]\n v_cndmask_b32_e64 v29, 0, v29, s[46:47]\n"
            "v_cvt_pk_bf16_f32 v28, v28, v29\n"
            "s_cmp_lg_u32 %[left], 0\n s_nop 0\n"
            "v_mov_b32_dpp v120, v28 quad_perm:[0,0,0,0] row_mask:0xf bank_mask:0xf\n"
            "v_mov_b32_dpp v121, v28 quad_perm:[1,1,1,1] row_mask:0xf bank_mask:0xf\n"
            "v_mov_b32_dpp v122, v28 quad_perm:[2,2,2,2] row_mask:0xf bank_mask:0xf\n"
            "v_mov_b32_dpp v123, v28 quad_perm:[3,3,3,3] row_mask:0xf bank_mask:0xf\n"
            "s_nop 1\n"
            PMF_(0) PMF_(1) PMF_(2) PMF_(3) PMF_(4) PMF_(5) PMF_(6) PMF_(7)
            "s_cbranch_scc0 4f\n"
            "s_sub_u32 %[left], %[left], 1\n"
            "s_branch 1b\n"
            "3:\n"
            DMA_BLK("%[wa]", "%[rv]")
            "s_branch 2b\n"
            "4:\n"
            "s_nop 1\n"
            TSTM(D)
            "s_waitcnt vmcnt(0)\n s_barrier\n"
            FL_(0) FL_(1) FL_(2) FL_(3) FL_(4) FL_(5) FL_(6) FL_(7)
            TSTM(E)
            : [left] "+&s"(left), [cur] "+&s"(cur), [nxt] "+&s"(nxt), [seq] "+&s"(seq), [rows] "+&s"(rows) TST_ARG
            : [lp] "v"(lp), [np] "v"(np), [d0] "v"(d0), [s0] "v"(buf + srl), [t0] "v"(t0), [t1] "v"(t1), [av] "v"(a),
              [qa] "v"(qrow + 16 * g), [fa] "v"(qrow + 8 * g), [wa] "v"(wa),
              [buf] "s"(buf), [rx] "s"(sx), [rv] "s"(sv), [c128] "s"(128), [msk] "s"(0x00ffff00)
            : "v24", CLOB_V, CLOB_S TST_CLOB);
    } else {
        auto rx = __builtin_amdgcn_make_buffer_rsrc((void*)x, 0, NN * 256, 0x00020000);
        float ac[4][2] = {};
        for (int w = 0; w < NP; w++) {
            const unsigned tw = ((const unsigned*)(ws + WS_TAB))[w * NB + b];
            const unsigned* sg = (const unsigned*)(ws + WS_ENT) + w * CH + (tw & 0xffffu);
            for (int i = 0; i < (int)(tw >> 16); i++) {
                const unsigned e = sg[i];
                if ((int)(e >> 28) != v) continue;
                const unsigned xv = __builtin_amdgcn_raw_buffer_load_b32(rx, (e & 0xffff00u) + lane * 4, 0, 0);
                const unsigned qv = *(const unsigned*)(lds + L_QS + v * QP + (e & 3) * QT + lane * 4);
                const float x0 = __builtin_bit_cast(float, xv << 16), x1 = __builtin_bit_cast(float, xv & 0xffff0000u);
                float s = x0 * __builtin_bit_cast(float, qv << 16) + x1 * __builtin_bit_cast(float, qv & 0xffff0000u);
#pragma unroll
                for (int o = 32; o > 0; o >>= 1) s += __shfl_xor(s, o, 64);
                const float al = (float)(__bf16)__builtin_amdgcn_rcpf(1.0f + __builtin_amdgcn_exp2f(s * -0.12751743f));
#pragma unroll
                for (int a = 0; a < 4; a++) {
                    const float aa = (int)(e & 3) == a ? al : 0.f;
                    ac[a][0] += aa * x0;
                    ac[a][1] += aa * x1;
                }
            }
        }
        __builtin_amdgcn_s_barrier();
#pragma unroll
        for (int a = 0; a < 4; a++) *(unsigned*)(lds + L_QS + v * QP + a * QT + lane * 4) = pk_bf16(ac[a][0], ac[a][1]);
        asm volatile("s_waitcnt vmcnt(0)\n s_mov_b32 m0, %[buf]\n s_nop 0\n" DMA_BLK("%[wa]", "%[rv]")
                     :: [wa] "v"(wa), [buf] "s"(buf), [rv] "s"(sv), [c128] "s"(128) : "scc", "memory");
    }
    {
        const unsigned tw0 = L_X + 8192 * (g >> 1) + trn;
        const unsigned orow = L_OUT + 8192 * ((32 * mh + m) >> 2) + 272 * (m & 3) + 64 * ((m >> 2) & 1);
        const unsigned node = t >> 4;
        const unsigned oa = node < NPB ? b * NPB * 256 + t * 16 : OOB;
        const i32x4 so = make_srd(out, NN * 256);
        asm volatile(
            "s_waitcnt vmcnt(0) lgkmcnt(0)\n"
            AST(6)
            "s_barrier\n"
            WTR_(0, "tw0", 0, 16384) BRD_(0, 64, 4368) WTR_(1, "tw0", 32768, 16384) BRD_(1, 64, 4368)
            WTR_(2, "tw1", 0, 16384) BRD_(2, 64, 4368) WTR_(3, "tw1", 32768, 16384) BRD_(3, 64, 4368)
            GEMM_MF
            "s_nop 6\n"
            "v_cvt_pk_bf16_f32 v112, v112, v113\n v_cvt_pk_bf16_f32 v113, v114, v115\n"
            "v_cvt_pk_bf16_f32 v116, v116, v117\n v_cvt_pk_bf16_f32 v117, v118, v119\n"
            "ds_write_b64 %[ow], v[112:113]\n ds_write_b64 %[ow], v[116:117] offset:32768\n"
            "s_waitcnt lgkmcnt(0)\n"
            "s_barrier\n"
            "ds_read_b128 v[28:31], %[orr]\n"
            "s_waitcnt lgkmcnt(0)\n"
            "buffer_store_dwordx4 v[28:31], %[oa], %[ro], 0 offen\n"
            AST(7)
            :: [tw0] "v"(tw0), [tw1] "v"(tw0 + 65536), [gb] "v"(L_QS + prow + g * 16), [ow] "v"(orow + nt * 32 + g * 8),
               [orr] "v"(L_OUT + 8192 * (node >> 2) + 272 * (node & 3) + 64 * ((node >> 2) & 1) + 16 * m), [oa] "v"(oa), [ro] "s"(so)
            : "v24", CLOB_V, CLOB_S CLOB_ST);
    }
}

#define ST1
#define ST2

extern "C" void kernel_launch(void* const* d_in, const int* in_sizes, int n_in, void* d_out, int out_size,
                              void* d_ws, size_t ws_size, hipStream_t stream) {
    const void* x = d_in[0];
    const int* src = (const int*)d_in[1];
    const int* dst = (const int*)d_in[2];
    const unsigned short* wq = (const unsigned short*)d_in[3];
    const unsigned short* wv = (const unsigned short*)d_in[4];
    k1_partition<<<2 * NP, 448, 0, stream>>>(src, dst, x, (char*)d_ws ST1);
    k2_aggregate<<<NB, 1024, 0, stream>>>(x, wq, wv, (const char*)d_ws, (char*)d_out ST2);
}
```

```cpp
#include <hip/hip_runtime.h>
#include <hip/hip_bf16.h>

typedef unsigned u32x4 __attribute__((ext_vector_type(4)));
typedef unsigned u32x2 __attribute__((ext_vector_type(2)));
typedef int i32x4 __attribute__((ext_vector_type(4)));
typedef float f32x4 __attribute__((ext_vector_type(4)));
typedef __bf16 bf16x8 __attribute__((ext_vector_type(8)));
typedef __bf16 bf16x2 __attribute__((ext_vector_type(2)));

constexpr int NN = 12500, EE = 400000;
constexpr int NB = 256;
constexpr int NPB = 49;
constexpr int NP = 256;
constexpr int CH = 1564;
constexpr int CAPW = 192;
constexpr unsigned OOB = 0x80000000u;
constexpr unsigned SENT = 0x00ffffffu;

constexpr unsigned WS_TAB = 0;
constexpr unsigned WS_ENT = WS_TAB + NP * NB * 4;
constexpr unsigned WS_END = WS_ENT + NP * CH * 4 + 256;

#define WARM1 8
constexpr int SLICE_LINES = 784;

#define STAMP(buf, slot) do { if (slot + 10 == 10 && (threadIdx.x & 63) == 0) ((unsigned long long*)(ws + (64 << 20)))[blockIdx.x * 16 + (threadIdx.x >> 6)] = __builtin_amdgcn_s_memrealtime(); } while (0)
#define STAMP_ARG

__device__ __forceinline__ unsigned pk_bf16(float a, float b) {
    bf16x2 v = {(__bf16)a, (__bf16)b};
    return __builtin_bit_cast(unsigned, v);
}
template <int CTRL, int ROWM = 0xf>
__device__ __forceinline__ int dpp_add_i(int v) {
    return v + __builtin_amdgcn_update_dpp(0, v, CTRL, ROWM, 0xf, false);
}
__device__ __forceinline__ int wave_incl_scan(int v) {
    v = dpp_add_i<0x111>(v);
    v = dpp_add_i<0x112>(v);
    v = dpp_add_i<0x114>(v);
    v = dpp_add_i<0x118>(v);
    v = dpp_add_i<0x142, 0xa>(v);
    v = dpp_add_i<0x143, 0xc>(v);
    return v;
}
__device__ __forceinline__ f32x4 mfma16(u32x4 a, u32x4 b, f32x4 c) {
    return __builtin_amdgcn_mfma_f32_16x16x32_bf16(__builtin_bit_cast(bf16x8, a), __builtin_bit_cast(bf16x8, b), c, 0, 0, 0);
}
__device__ __forceinline__ i32x4 make_srd(const void* p, unsigned bytes) {
    const unsigned long long a = (unsigned long long)p;
    return i32x4{(int)a, (int)(a >> 32) & 0xffff, (int)bytes, 0x00020000};
}

__global__ __launch_bounds__(448) void k1_partition(const int* __restrict__ src, const int* __restrict__ dst,
                                                   const void* __restrict__ x, char* __restrict__ ws STAMP_ARG) {
    __shared__ __attribute__((aligned(16))) unsigned hist[NB];
    __shared__ __attribute__((aligned(16))) unsigned offs[NB];
    __shared__ __attribute__((aligned(16))) unsigned sorted[CH + 4];
    unsigned long long t_top; asm volatile("s_memrealtime %0\n s_waitcnt lgkmcnt(0)" : "=s"(t_top) :: "memory"); __builtin_amdgcn_sched_barrier(0);
    if ((threadIdx.x & 63) == 0) ((unsigned long long*)(ws + (64 << 20)))[blockIdx.x * 16 + (threadIdx.x >> 6)] = t_top;
    const int t = threadIdx.x, w = blockIdx.x;
    if (w >= NP) {
        const int hw = w - NP;
        auto rx = __builtin_amdgcn_make_buffer_rsrc((void*)x, 0, NN * 256, 0x00020000);
        constexpr int L1 = SLICE_LINES * WARM1 / 16;
        unsigned warm = 0;
#pragma unroll
        for (int o = 0; o < L1; o += 448)
            warm ^= __builtin_amdgcn_raw_buffer_load_b32(rx, o + t < L1 ? ((hw >> 3) * SLICE_LINES + o + t) * 128 : OOB, 0, 0);
        asm volatile("" ::"v"(warm));
        STAMP(stamps, 5);
        return;
    }
    const int e0 = w * CH + t * 4;
    const bool act = t < CH / 4 && e0 < EE;
    u32x4 d4 = {0, 0, 0, 0}, s4 = {0, 0, 0, 0};
    if (act) {
        d4 = *(const u32x4*)(dst + e0);
        s4 = *(const u32x4*)(src + e0);
    }
    if (t < NB) hist[t] = 0;
    __syncthreads();
    STAMP(stamps, 1);
    unsigned b[4], r[4], ent[4];
    if (act) {
#pragma unroll
        for (int j = 0; j < 4; j++) {
            const unsigned d = d4[j];
            b[j] = min((d * 21400u) >> 20, (unsigned)(NB - 1));
            r[j] = atomicAdd(&hist[b[j]], 1u);
            const unsigned dl = d - b[j] * NPB;
            ent[j] = dl << 28 | (s4[j] & 0xffffu) << 8 | (dl >> 4 & 3);
        }
    }
    __syncthreads();
    STAMP(stamps, 2);
    {
        const int l = t & 63;
        const u32x4 c = *(const u32x4*)&hist[l * 4];
        const int tot = c.x + c.y + c.z + c.w;
        const unsigned ex = wave_incl_scan(tot) - tot;
        const u32x4 o = {ex, ex + c.x, ex + c.x + c.y, ex + c.x + c.y + c.z};
        *(volatile u32x4*)&offs[l * 4] = o;
        if (t >= 384) *(u32x4*)(ws + WS_TAB + (w * NB + l * 4) * 4) = o | c << 16;
    }
    __builtin_amdgcn_wave_barrier();
    if (act) {
#pragma unroll
        for (int j = 0; j < 4; j++) sorted[((volatile unsigned*)offs)[b[j]] + r[j]] = ent[j];
    }
    __syncthreads();
    STAMP(stamps, 3);
    if (act) *(u32x4*)(ws + WS_ENT + e0 * 4) = *(const u32x4*)&sorted[t * 4];
    STAMP(stamps, 4);
}

constexpr int QP = 272, QT = 16 * QP + 16;
constexpr int L_CNT = 0;
constexpr int L_LIST = 64;
constexpr int L_QS = L_LIST + 16 * CAPW * 4;
constexpr int L_X = (L_QS + 4 * QT + 255) / 256 * 256;
constexpr int L_WQ = L_X + 32768;
constexpr int L_OUT = L_X + 4096;
constexpr int L_END = L_X + 16 * 8192;

#define PRIO(r) "s_setprio 0\n s_cmp_lt_i32 " r ", 1\n s_cbranch_scc1 8f\n s_setprio 1\n s_cmp_lt_i32 " r ", 2\n s_cbranch_scc1 8f\n" \
                "s_setprio 2\n s_cmp_lt_i32 " r ", 3\n s_cbranch_scc1 8f\n s_setprio 3\n 8:\n"
#define STR_(x) #x
#define STR(x) STR_(x)
#define DMA_BLK(ent, srd) \
    "buffer_load_dwordx4 " ent ", " srd ", 0 offen lds\n" \
    "s_add_u32 m0, m0, 0x400\n s_nop 0\n" \
    "buffer_load_dwordx4 " ent ", " srd ", %[c128] offen lds\n" \
    "s_add_u32 m0, m0, 0x400\n"
#define SRD_(i) "ds_read_b128 v[32+4*" #i ":35+4*" #i "], %[s0] offset:512*(" #i "/2)+4096*(" #i "&1)\n"
#define TRD_(i, a) "ds_read_b64_tr_b16 v[32+4*" #i ":33+4*" #i "], %[" a "] offset:512*(" #i "/2)\n" \
                   "ds_read_b64_tr_b16 v[34+4*" #i ":35+4*" #i "], %[" a "] offset:512*(" #i "/2)+4096\n"
#define SMF_(i, n, d, c) "s_waitcnt lgkmcnt(" #n ")\n v_mfma_f32_16x16x32_bf16 " d ", v[32+4*" #i ":35+4*" #i "], v[96+4*(" #i "/2):99+4*(" #i "/2)], " c "\n"
#define PMF_(i) "v_mfma_f32_16x16x32_bf16 v[64+4*" #i ":67+4*" #i "], v[32+4*" #i ":35+4*" #i "], v[120:123], v[64+4*" #i ":67+4*" #i "]\n"
#define FL_(i) "v_cvt_pk_bf16_f32 v[32+2*" #i "], v[64+4*" #i "], v[65+4*" #i "]\n v_cvt_pk_bf16_f32 v[33+2*" #i "], v[66+4*" #i "], v[67+4*" #i "]\n" \
               "ds_write_b64 %[fa], v[32+2*" #i ":33+2*" #i "] offset:32*" #i "\n"
#define WTR_(s, a, o, h) "ds_read_b64_tr_b16 v[96+4*" #s ":97+4*" #s "], %[" a "] offset:" #o "\n" \
                         "ds_read_b64_tr_b16 v[98+4*" #s ":99+4*" #s "], %[" a "] offset:" #o "+" #h "\n"
#define BRD_(s, ks, km) "ds_read_b128 v[32+8*" #s ":35+8*" #s "], %[gb] offset:" #ks "*" #s "\n" \
                        "ds_read_b128 v[36+8*" #s ":39+8*" #s "], %[gb] offset:" #ks "*" #s "+" #km "\n"
#define GMF_(s, n, c0, c1) "s_waitcnt lgkmcnt(" #n ")\n" \
    "v_mfma_f32_16x16x32_bf16 v[112:115], v[96+4*" #s ":99+4*" #s "], v[32+8*" #s ":35+8*" #s "], " c0 "\n" \
    "v_mfma_f32_16x16x32_bf16 v[116:119], v[96+4*" #s ":99+4*" #s "], v[36+8*" #s ":39+8*" #s "], " c1 "\n"
#define GEMM_MF GMF_(0, 12, "0", "0") GMF_(1, 8, "v[112:115]", "v[116:119]") GMF_(2, 4, "v[112:115]", "v[116:119]") GMF_(3, 0, "v[112:115]", "v[116:119]")
#define V10(a) "v" STR(a) "0", "v" STR(a) "1", "v" STR(a) "2", "v" STR(a) "3", "v" STR(a) "4", "v" STR(a) "5", "v" STR(a) "6", "v" STR(a) "7", "v" STR(a) "8", "v" STR(a) "9"
#define CLOB_V "v25", "v26", "v27", "v28", "v29", V10(3), V10(4), V10(5), V10(6), V10(7), V10(8), V10(9), V10(10), V10(11), "v120", "v121", "v122", "v123"
#define CLOB_S "s40", "s41", "s42", "s43", "s44", "s45", "s46", "s47", "vcc", "scc", "memory"
#define AST(k) ".if " #k " == 10\n s_memrealtime s[60:61]\n s_waitcnt lgkmcnt(0)\n .endif\n"
#define CLOB_ST , "s60", "s61"
#define TSTM(x)
#define TST_INIT
#define TST_ARG
#define TST_CLOB

__global__ __launch_bounds__(1024) void k2_aggregate(const void* __restrict__ x, const void* __restrict__ wq, const void* __restrict__ wv,
                                                    const char* __restrict__ ws, char* __restrict__ out STAMP_ARG) {
    __shared__ __attribute__((aligned(256))) char lds[L_END];
    int t = threadIdx.x;
    const int v = __builtin_amdgcn_readfirstlane(t >> 6);
    const int b = (blockIdx.x & 7) * 32 + (blockIdx.x >> 3);
    const i32x4 sx = make_srd(x, NN * 256), sw = make_srd(ws, WS_END), sq = make_srd(wq, 32768), sv = make_srd(wv, 32768);
    const unsigned d0 = 64 * ((t >> 5) & 1) + 16 * ((t & 3) ^ ((t >> 3) & 2));
    const unsigned wa = (32 * (v >> 2) + 16 * (v & 1) + 4 * ((v >> 1) & 1) + 8 * ((t >> 4) & 1) + ((t >> 2) & 3)) * 256 + d0;
    unsigned tv;
    {
        const unsigned ta = WS_TAB + ((t >> 2) * NB + b) * 4;
        const unsigned xa = (b * NPB + 8 * (v >> 1) + ((t >> 2) & 7)) * 256 + d0 + 128 * (v & 1);
        asm volatile(
            AST(0)
            "buffer_load_dword v24, %[ta], %[rw], 0 offen\n"
            "s_mov_b32 m0, %[mx]\n s_nop 0\n"
            "buffer_load_dwordx4 %[xa], %[rx], 0 offen lds\n"
            "s_mov_b32 m0, %[mw]\n s_nop 0\n"
            DMA_BLK("%[wa]", "%[rq]")
            : "={v24}"(tv)
            : [ta] "v"(ta), [xa] "v"(xa), [wa] "v"(wa), [rw] "s"(sw), [rx] "s"(sx), [rq] "s"(sq), [c128] "s"(128),
              [mx] "s"(L_X + 1024 * v), [mw] "s"(L_WQ + 2048 * v)
            : "scc", "memory" CLOB_ST);
    }
    asm volatile("" : "+v"(t));
    const int lane = t & 63, g = lane >> 4, m = t & 15, nt = v & 7, mh = v >> 3;
    if (t < 16 * CAPW / 4) *(u32x4*)(lds + L_LIST + t * 16) = u32x4{SENT, SENT, SENT, SENT};
    else if (t < 16 * CAPW / 4 + 16) *(unsigned*)(lds + L_CNT + m * 4) = L_LIST + m * CAPW * 4;
    const unsigned lbase = L_LIST + v * CAPW * 4;
    const unsigned buf = L_X + v * 8192;
    const unsigned trl = 256 * (g & 1) + 64 * (m >> 2) + 16 * (((t >> 1) & 1) ^ (2 * (g & 1))) + 8 * (t & 1);
    const unsigned trn = (trl ^ (32 * (nt & 1))) + 512 * (nt >> 1);
    const unsigned srl = 2048 * (m >> 3) + 64 * (m & 7) + 16 * (g ^ ((m >> 1) & 2));
    const unsigned prow = 2 * mh * QT + m * QP;
    const unsigned lp = lbase + ((t >> 2) & 7) * 4;
    unsigned cnt_l, cnt_v;
    {
        const unsigned ei = (t & 3) * 4;
        const unsigned eb = WS_ENT + ((t >> 2) * CH + ei) * 4;
        const unsigned gb = L_X + 8192 * mh + srl;
        asm volatile(
            "v_bfrev_b32 v27, 1\n"
            "s_waitcnt vmcnt(3)\n"
            AST(1)
            "v_and_b32 v25, 0xffff, v24\n"
            "v_sub_u32_sdwa v24, v24, %[ei] dst_sel:DWORD dst_unused:UNUSED_PAD src0_sel:WORD_1 src1_sel:DWORD\n"
            "v_lshl_add_u32 v25, v25, 2, %[eb]\n"
            "v_cmp_lt_i32_e64 s[40:41], 0, v24\n v_cmp_lt_i32_e64 s[42:43], 1, v24\n"
            "v_cmp_lt_i32_e64 s[44:45], 2, v24\n v_cmp_lt_i32_e64 s[46:47], 3, v24\n"
            "v_cndmask_b32_e64 v27, v27, v25, s[40:41]\n"
            "buffer_load_dwordx4 v[28:31], v27, %[rw], 0 offen\n"
            "s_waitcnt vmcnt(1) lgkmcnt(0)\n"
            "s_barrier\n"
            AST(2)
            WTR_(0, "tq", 0, 4096) BRD_(0, 512, 4096) WTR_(1, "tq", 8192, 4096) BRD_(1, 512, 4096)
            WTR_(2, "tq", 16384, 4096) BRD_(2, 512, 4096) WTR_(3, "tq", 24576, 4096) BRD_(3, 512, 4096)
            GEMM_MF
            "v_mov_b32 v26, 4\n"
            "s_nop 5\n"
            "v_cvt_pk_bf16_f32 v112, v112, v113\n v_cvt_pk_bf16_f32 v113, v114, v115\n"
            "v_cvt_pk_bf16_f32 v116, v116, v117\n v_cvt_pk_bf16_f32 v117, v118, v119\n"
            "ds_write_b64 %[qw], v[112:113]\n ds_write_b64 %[qw], v[116:117] offset:4368\n"
            AST(3)
            "5:\n"
            "s_waitcnt vmcnt(0)\n"
            "v_lshrrev_b32 v120, 26, v28\n v_lshrrev_b32 v121, 26, v29\n v_lshrrev_b32 v122, 26, v30\n v_lshrrev_b32 v123, 26, v31\n"
            "s_mov_b64 exec, s[40:41]\n ds_add_rtn_u32 v120, v120, v26\n"
            "s_mov_b64 exec, s[42:43]\n ds_add_rtn_u32 v121, v121, v26\n"
            "s_mov_b64 exec, s[44:45]\n ds_add_rtn_u32 v122, v122, v26\n"
            "s_mov_b64 exec, s[46:47]\n ds_add_rtn_u32 v123, v123, v26\n"
            "s_mov_b64 exec, -1\n"
            "v_cmp_lt_i32 vcc, 16, v24\n"
            "s_waitcnt lgkmcnt(0)\n"
            "v_min_u32 v120, %[lmax], v120\n v_min_u32 v121, %[lmax], v121\n v_min_u32 v122, %[lmax], v122\n v_min_u32 v123, %[lmax], v123\n"
            "s_mov_b64 exec, s[40:41]\n ds_write_b32 v120, v28\n"
            "s_mov_b64 exec, s[42:43]\n ds_write_b32 v121, v29\n"
            "s_mov_b64 exec, s[44:45]\n ds_write_b32 v122, v30\n"
            "s_mov_b64 exec, s[46:47]\n ds_write_b32 v123, v31\n"
            "s_mov_b64 exec, -1\n"
            "s_cbranch_vccnz 6f\n"
            "s_waitcnt lgkmcnt(0)\n"
            AST(4)
            "s_barrier\n"
            "ds_read_b32 v24, %[ca]\n"
            "ds_read2_b32 v[28:29], %[lp] offset0:0 offset1:8\n"
            "ds_read2_b32 v[30:31], %[lp] offset0:16 offset1:24\n"
            "s_mov_b32 m0, %[buf]\n"
            "s_waitcnt lgkmcnt(2)\n"
            "v_readlane_b32 %[cnt], v24, %[wv]\n"
            "s_waitcnt lgkmcnt(0)\n"
            "v_and_or_b32 v28, v28, %[msk], %[d0]\n v_and_or_b32 v29, v29, %[msk], %[d0]\n"
            "v_and_or_b32 v30, v30, %[msk], %[d0]\n v_and_or_b32 v31, v31, %[msk], %[d0]\n"
            "s_sub_i32 %[cnt], %[cnt], %[lb4]\n"
            "s_max_i32 %[cnt], %[cnt], 0\n"
            "s_lshr_b32 s40, %[cnt], 7\n"
            PRIO("s40")
            DMA_BLK("v28", "%[rx]") "s_nop 0\n" DMA_BLK("v29", "%[rx]") "s_nop 0\n"
            DMA_BLK("v30", "%[rx]") "s_nop 0\n" DMA_BLK("v31", "%[rx]")
            "s_branch 7f\n"
            "6:\n"
            "v_add_u32 v24, -16, v24\n v_add_u32 v25, 64, v25\n v_bfrev_b32 v27, 1\n"
            "v_cmp_lt_i32_e64 s[40:41], 0, v24\n v_cmp_lt_i32_e64 s[42:43], 1, v24\n"
            "v_cmp_lt_i32_e64 s[44:45], 2, v24\n v_cmp_lt_i32_e64 s[46:47], 3, v24\n"
            "s_waitcnt lgkmcnt(0)\n"
            "v_cndmask_b32_e64 v27, v27, v25, s[40:41]\n"
            "buffer_load_dwordx4 v[28:31], v27, %[rw], 0 offen\n"
            "s_branch 5b\n"
            "7:\n"
            AST(5)
            : "+{v24}"(tv), [cnt] "=&s"(cnt_v)
            : [ei] "v"(ei), [eb] "v"(eb), [gb] "v"(gb), [tq] "v"(L_WQ + 2048 * (g >> 1) + trn), [qw] "v"(L_QS + prow + nt * 32 + g * 8),
              [lp] "v"(lp), [ca] "v"(L_CNT + m * 4), [d0] "v"(d0), [wv] "s"(v), [lb4] "s"(lbase + 4),
              [rw] "s"(sw), [rx] "s"(sx), [c128] "s"(128), [msk] "s"(0x00ffff00), [buf] "s"(buf), [lmax] "s"(L_QS - 4)
            : CLOB_V, CLOB_S CLOB_ST);
        cnt_l = tv - (L_LIST + m * CAPW * 4);
        cnt_v = __builtin_amdgcn_readfirstlane(cnt_v);
    }
    const bool generic = __ballot(cnt_l > CAPW * 4) != 0;
    if (!generic) {
        const int a = m >> 2, bq = t & 3;
        const unsigned qrow = L_QS + v * QP + a * QT;
        const unsigned np = lbase + (16 * (bq >> 1) + 4 * g + 2 * (bq & 1)) * 4;
        const unsigned t0 = buf + 2048 * (g >> 1) + trl, t1 = t0 ^ 32;
        unsigned left = cnt_v >> 7, cur = 0, nxt = cnt_v & ~127u, seq = 128, rows = cnt_v & 127;
        asm volatile(
            TST_INIT TSTM(S)
            "ds_read_b128 v[96:99], %[qa]\n ds_read_b128 v[100:103], %[qa] offset:64\n"
            "ds_read_b128 v[104:107], %[qa] offset:128\n ds_read_b128 v[108:111], %[qa] offset:192\n"
            ".irp i,64,65,66,67,68,69,70,71,72,73,74,75,76,77,78,79,80,81,82,83,84,85,86,87,88,89,90,91,92,93,94,95\n v_mov_b32 v\\i, 0\n .endr\n"
            "s_mov_b32 s40, 0xaaaaaaaa\n s_mov_b32 s41, s40\n s_mov_b32 s42, 0xcccccccc\n s_mov_b32 s43, s42\n"
            "s_sub_i32 s44, %[left], 1\n"
            PRIO("s44")
            "1:\n"
            "v_add_u32 v30, %[cur], %[np]\n v_add_u32 v31, %[nxt], %[lp]\n"
            "ds_read_b64 v[28:29], v30\n"
            "ds_read2_b32 v[24:25], v31 offset0:0 offset1:8\n"
            "ds_read2_b32 v[26:27], v31 offset0:16 offset1:24\n"
            "s_mov_b32 %[cur], %[nxt]\n s_mov_b32 %[nxt], %[seq]\n s_add_u32 %[seq], %[seq], 0x80\n"
            "s_waitcnt vmcnt(0)\n"
            TSTM(A)
            SRD_(0) SRD_(1) SRD_(2) SRD_(3) SRD_(4) SRD_(5) SRD_(6) SRD_(7)
            SMF_(0, 7, "v[112:115]", "0") TRD_(0, "t0")
            SMF_(1, 8, "v[116:119]", "0") TRD_(1, "t1")
            SMF_(2, 9, "v[112:115]", "v[112:115]") TRD_(2, "t0")
            SMF_(3, 10, "v[116:119]", "v[116:119]") TRD_(3, "t1")
            SMF_(4, 11, "v[112:115]", "v[112:115]") TRD_(4, "t0")
            SMF_(5, 12, "v[116:119]", "v[116:119]") TRD_(5, "t1")
            SMF_(6, 13, "v[112:115]", "v[112:115]") TRD_(6, "t0")
            SMF_(7, 14, "v[116:119]", "v[116:119]") TRD_(7, "t1")
            "v_and_or_b32 v24, v24, %[msk], %[d0]\n v_and_or_b32 v25, v25, %[msk], %[d0]\n"
            "v_and_or_b32 v26, v26, %[msk], %[d0]\n v_and_or_b32 v27, v27, %[msk], %[d0]\n"
            "v_cmp_eq_u32_sdwa s[44:45], v28, %[av] src0_sel:BYTE_0 src1_sel:DWORD\n"
            "v_cmp_eq_u32_sdwa s[46:47], v29, %[av] src0_sel:BYTE_0 src1_sel:DWORD\n"
            "v_cndmask_b32_e64 v28, v112, v114, s[40:41]\n v_cndmask_b32_e64 v29, v113, v115, s[40:41]\n"
            "v_cndmask_b32_e64 v30, v116, v118, s[40:41]\n v_cndmask_b32_e64 v31, v117, v119, s[40:41]\n"
            "v_cndmask_b32_e64 v28, v28, v30, s[42:43]\n v_cndmask_b32_e64 v29, v29, v31, s[42:43]\n"
            "v_mul_f32 v28, 0xbe0293ee, v28\n v_mul_f32 v29, 0xbe0293ee, v29\n"
            "v_exp_f32 v28, v28\n v_exp_f32 v29, v29\n"
            TSTM(B)
            "s_mov_b32 m0, %[buf]\n"
            "s_cmp_eq_u32 %[left], 0\n"
            "s_waitcnt lgkmcnt(0)\n"
            "s_cbranch_scc1 3f\n"
            DMA_BLK("v24", "%[rx]") "s_cmp_lt_u32 %[rows], 32\n s_cbranch_scc1 9f\n"
            DMA_BLK("v25", "%[rx]") "s_cmp_lt_u32 %[rows], 64\n s_cbranch_scc1 9f\n"
            DMA_BLK("v26", "%[rx]") "s_cmp_lt_u32 %[rows], 96\n s_cbranch_scc1 9f\n"
            DMA_BLK("v27", "%[rx]")
            "9:\n"
            "s_movk_i32 %[rows], 0x7f\n"
            "s_sub_i32 vcc_lo, %[left], 2\n"
            PRIO("vcc_lo")
            "2:\n"
            TSTM(C)
            "v_add_f32 v28, 1.0, v28\n v_add_f32 v29, 1.0, v29\n"
            "v_rcp_f32 v28, v28\n v_rcp_f32 v29, v29\n"
            "s_nop 0\n"
            "v_cndmask_b32_e64 v28, 0, v28, s[44:45]\n v_cndmask_b32_e64 v29, 0, v29, s[46:47]\n"
            "v_cvt_pk_bf16_f32 v28, v28, v29\n"
            "s_cmp_lg_u32 %[left], 0\n s_nop 0\n"
            "v_mov_b32_dpp v120, v28 quad_perm:[0,0,0,0] row_mask:0xf bank_mask:0xf\n"
            "v_mov_b32_dpp v121, v28 quad_perm:[1,1,1,1] row_mask:0xf bank_mask:0xf\n"
            "v_mov_b32_dpp v122, v28 quad_perm:[2,2,2,2] row_mask:0xf bank_mask:0xf\n"
            "v_mov_b32_dpp v123, v28 quad_perm:[3,3,3,3] row_mask:0xf bank_mask:0xf\n"
            "s_nop 1\n"
            PMF_(0) PMF_(1) PMF_(2) PMF_(3) PMF_(4) PMF_(5) PMF_(6) PMF_(7)
            "s_cbranch_scc0 4f\n"
            "s_sub_u32 %[left], %[left], 1\n"
            "s_branch 1b\n"
            "3:\n"
            DMA_BLK("%[wa]", "%[rv]")
            "s_branch 2b\n"
            "4:\n"
            "s_nop 1\n"
            TSTM(D)
            "s_waitcnt vmcnt(0)\n s_barrier\n"
            FL_(0) FL_(1) FL_(2) FL_(3) FL_(4) FL_(5) FL_(6) FL_(7)
            TSTM(E)
            : [left] "+&s"(left), [cur] "+&s"(cur), [nxt] "+&s"(nxt), [seq] "+&s"(seq), [rows] "+&s"(rows) TST_ARG
            : [lp] "v"(lp), [np] "v"(np), [d0] "v"(d0), [s0] "v"(buf + srl), [t0] "v"(t0), [t1] "v"(t1), [av] "v"(a),
              [qa] "v"(qrow + 16 * g), [fa] "v"(qrow + 8 * g), [wa] "v"(wa),
              [buf] "s"(buf), [rx] "s"(sx), [rv] "s"(sv), [c128] "s"(128), [msk] "s"(0x00ffff00)
            : "v24", CLOB_V, CLOB_S TST_CLOB);
    } else {
        auto rx = __builtin_amdgcn_make_buffer_rsrc((void*)x, 0, NN * 256, 0x00020000);
        float ac[4][2] = {};
        for (int w = 0; w < NP; w++) {
            const unsigned tw = ((const unsigned*)(ws + WS_TAB))[w * NB + b];
            const unsigned* sg = (const unsigned*)(ws + WS_ENT) + w * CH + (tw & 0xffffu);
            for (int i = 0; i < (int)(tw >> 16); i++) {
                const unsigned e = sg[i];
                if ((int)(e >> 28) != v) continue;
                const unsigned xv = __builtin_amdgcn_raw_buffer_load_b32(rx, (e & 0xffff00u) + lane * 4, 0, 0);
                const unsigned qv = *(const unsigned*)(lds + L_QS + v * QP + (e & 3) * QT + lane * 4);
                const float x0 = __builtin_bit_cast(float, xv << 16), x1 = __builtin_bit_cast(float, xv & 0xffff0000u);
                float s = x0 * __builtin_bit_cast(float, qv << 16) + x1 * __builtin_bit_cast(float, qv & 0xffff0000u);
#pragma unroll
                for (int o = 32; o > 0; o >>= 1) s += __shfl_xor(s, o, 64);
                const float al = (float)(__bf16)__builtin_amdgcn_rcpf(1.0f + __builtin_amdgcn_exp2f(s * -0.12751743f));
#pragma unroll
                for (int a = 0; a < 4; a++) {
                    const float aa = (int)(e & 3) == a ? al : 0.f;
                    ac[a][0] += aa * x0;
                    ac[a][1] += aa * x1;
                }
            }
        }
        __builtin_amdgcn_s_barrier();
#pragma unroll
        for (int a = 0; a < 4; a++) *(unsigned*)(lds + L_QS + v * QP + a * QT + lane * 4) = pk_bf16(ac[a][0], ac[a][1]);
        asm volatile("s_waitcnt vmcnt(0)\n s_mov_b32 m0, %[buf]\n s_nop 0\n" DMA_BLK("%[wa]", "%[rv]")
                     :: [wa] "v"(wa), [buf] "s"(buf), [rv] "s"(sv), [c128] "s"(128) : "scc", "memory");
    }
    {
        const unsigned tw0 = L_X + 8192 * (g >> 1) + trn;
        const unsigned orow = L_OUT + 8192 * ((32 * mh + m) >> 2) + 272 * (m & 3) + 64 * ((m >> 2) & 1);
        const unsigned node = t >> 4;
        const unsigned oa = node < NPB ? b * NPB * 256 + t * 16 : OOB;
        const i32x4 so = make_srd(out, NN * 256);
        asm volatile(
            "s_waitcnt vmcnt(0) lgkmcnt(0)\n"
            AST(6)
            "s_barrier\n"
            WTR_(0, "tw0", 0, 16384) BRD_(0, 64, 4368) WTR_(1, "tw0", 32768, 16384) BRD_(1, 64, 4368)
            WTR_(2, "tw1", 0, 16384) BRD_(2, 64, 4368) WTR_(3, "tw1", 32768, 16384) BRD_(3, 64, 4368)
            GEMM_MF
            "s_nop 6\n"
            "v_cvt_pk_bf16_f32 v112, v112, v113\n v_cvt_pk_bf16_f32 v113, v114, v115\n"
            "v_cvt_pk_bf16_f32 v116, v116, v117\n v_cvt_pk_bf16_f32 v117, v118, v119\n"
            "ds_write_b64 %[ow], v[112:113]\n ds_write_b64 %[ow], v[116:117] offset:32768\n"
            "s_waitcnt lgkmcnt(0)\n"
            "s_barrier\n"
            "ds_read_b128 v[28:31], %[orr]\n"
            "s_waitcnt lgkmcnt(0)\n"
            "buffer_store_dwordx4 v[28:31], %[oa], %[ro], 0 offen\n"
            AST(7)
            :: [tw0] "v"(tw0), [tw1] "v"(tw0 + 65536), [gb] "v"(L_QS + prow + g * 16), [ow] "v"(orow + nt * 32 + g * 8),
               [orr] "v"(L_OUT + 8192 * (node >> 2) + 272 * (node & 3) + 64 * ((node >> 2) & 1) + 16 * m), [oa] "v"(oa), [ro] "s"(so)
            : "v24", CLOB_V, CLOB_S CLOB_ST);
    }
    if (blockIdx.x < 8 && v == 0) { unsigned long long a = __builtin_amdgcn_s_memrealtime(); while (__builtin_amdgcn_s_memrealtime() - a < 400) __builtin_amdgcn_s_sleep(1); asm volatile("buffer_wbl2 sc1\n s_waitcnt vmcnt(0)" ::: "memory"); }
    if (v < 7) { unsigned long long a = ((const volatile unsigned long long*)(ws + (64 << 20)))[0]; while (__builtin_amdgcn_s_memrealtime() - a < 3000) __builtin_amdgcn_s_sleep(1); }
}

#define ST1
#define ST2

extern "C" void kernel_launch(void* const* d_in, const int* in_sizes, int n_in, void* d_out, int out_size,
                              void* d_ws, size_t ws_size, hipStream_t stream) {
    const void* x = d_in[0];
    const int* src = (const int*)d_in[1];
    const int* dst = (const int*)d_in[2];
    const unsigned short* wq = (const unsigned short*)d_in[3];
    const unsigned short* wv = (const unsigned short*)d_in[4];
    k1_partition<<<2 * NP, 448, 0, stream>>>(src, dst, x, (char*)d_ws ST1);
    k2_aggregate<<<NB, 1024, 0, stream>>>(x, wq, wv, (const char*)d_ws, (char*)d_out ST2);
}
```

```cpp
#include <hip/hip_runtime.h>
#include <hip/hip_bf16.h>

typedef unsigned u32x4 __attribute__((ext_vector_type(4)));
typedef unsigned u32x2 __attribute__((ext_vector_type(2)));
typedef int i32x4 __attribute__((ext_vector_type(4)));
typedef __bf16 bf16x2 __attribute__((ext_vector_type(2)));

constexpr int NN = 12500, EE = 400000;
constexpr int NB = 256;
constexpr int NPB = 49;
constexpr int CH = 1564;
constexpr int CAPW = 192;
constexpr unsigned OOB = 0x80000000u;
constexpr unsigned SENT = 0x00ffffffu;
constexpr int SC1 = 16;

constexpr unsigned WS_EPOCH = 0;
constexpr unsigned WS_TAB = 256;
constexpr unsigned WS_ENT = WS_TAB + NB * NB * 8;
constexpr unsigned WS_END = WS_ENT + NB * CH * 4 + 256;

#define NWARM 384

__device__ __forceinline__ unsigned pk_bf16(float a, float b) {
    bf16x2 v = {(__bf16)a, (__bf16)b};
    return __builtin_bit_cast(unsigned, v);
}
__device__ __forceinline__ i32x4 make_srd(const void* p, unsigned bytes) {
    const unsigned long long a = (unsigned long long)p;
    return i32x4{(int)a, (int)(a >> 32) & 0xffff, (int)bytes, 0x00020000};
}

constexpr int QP = 288, QT = 16 * QP + 16;
constexpr int L_CNT = 0;
constexpr int L_LIST = 64;
constexpr int L_QS = L_LIST + 16 * CAPW * 4;
constexpr int L_X = (L_QS + 4 * QT + 255) / 256 * 256;
constexpr int L_WQ = L_X + 16384, L_WV = L_WQ + 32768;
constexpr int L_OUT = L_X;
constexpr int L_END = L_X + 16 * 8192;
#define CAPW_ 192
#define L_LIST_ 64
#define L_HIST_ 12352
#define L_OFFS_ 13376
#define L_SORT_ 14400
#define WS_ENT_ 524544
static_assert(CAPW_ == CAPW && L_LIST_ == L_LIST && L_HIST_ == L_QS && WS_ENT_ == WS_ENT && L_SORT_ + CH * 4 + 1024 <= L_X, "asm literals");

#define PRIO(r) "s_setprio 0\n s_cmp_lt_i32 " r ", 1\n s_cbranch_scc1 8f\n s_setprio 1\n s_cmp_lt_i32 " r ", 2\n s_cbranch_scc1 8f\n" \
                "s_setprio 2\n s_cmp_lt_i32 " r ", 3\n s_cbranch_scc1 8f\n s_setprio 3\n 8:\n"
#define OUTPOL " sc0 sc1 nt"
#define STR_(x) #x
#define STR(x) STR_(x)
#define DMA2(ent, srd, o0, o1) \
    "buffer_load_dwordx4 " ent ", " srd ", " o0 " offen lds\n" \
    "s_add_u32 m0, m0, 0x400\n s_nop 0\n" \
    "buffer_load_dwordx4 " ent ", " srd ", " o1 " offen lds\n" \
    "s_add_u32 m0, m0, 0x400\n"
#define DMA_BLK(ent) DMA2(ent, "%[rx]", "0", "%[c128]")
#define SRD_(i) "ds_read_b128 v[32+4*" #i ":35+4*" #i "], %[s0] offset:512*(" #i "/2)+4096*(" #i "&1)\n"
#define TRD_(i, a) "ds_read_b64_tr_b16 v[32+4*" #i ":33+4*" #i "], %[" a "] offset:512*(" #i "/2)\n" \
                   "ds_read_b64_tr_b16 v[34+4*" #i ":35+4*" #i "], %[" a "] offset:512*(" #i "/2)+4096\n"
#define SMF_(i, n, d, c) "s_waitcnt lgkmcnt(" #n ")\n v_mfma_f32_16x16x32_bf16 " d ", v[32+4*" #i ":35+4*" #i "], v[96+4*(" #i "/2):99+4*(" #i "/2)], " c "\n"
#define PMF_(i) "v_mfma_f32_16x16x32_bf16 v[64+4*" #i ":67+4*" #i "], v[32+4*" #i ":35+4*" #i "], v[120:123], v[64+4*" #i ":67+4*" #i "]\n"
#define CVT_(i) "v_cvt_pk_bf16_f32 v[32+2*" #i "], v[64+4*" #i "], v[65+4*" #i "]\n v_cvt_pk_bf16_f32 v[33+2*" #i "], v[66+4*" #i "], v[67+4*" #i "]\n"
#define SEL_(i) "v_cndmask_b32_e64 v[32+" #i "], v[32+" #i "], v[36+" #i "], s[40:41]\n v_cndmask_b32_e64 v[40+" #i "], v[40+" #i "], v[44+" #i "], s[40:41]\n" \
                "v_cndmask_b32_e64 v[32+" #i "], v[32+" #i "], v[40+" #i "], s[42:43]\n"
#define WTR_(r, s, o) "ds_read_b64_tr_b16 v[" #r "+4*" #s ":" #r "+1+4*" #s "], %[tq] offset:" #o "+8192*" #s "\n" \
                      "ds_read_b64_tr_b16 v[" #r "+2+4*" #s ":" #r "+3+4*" #s "], %[tq] offset:" #o "+8192*" #s "+4096\n"
#define BRD_(s, ks, km) "ds_read_b128 v[32+8*" #s ":35+8*" #s "], %[gb] offset:" #ks "*" #s "\n" \
                        "ds_read_b128 v[36+8*" #s ":39+8*" #s "], %[gb] offset:" #ks "*" #s "+" #km "\n"
#define GMF_(r, s, n, c0, c1) "s_waitcnt lgkmcnt(" #n ")\n" \
    "v_mfma_f32_16x16x32_bf16 v[112:115], v[" #r "+4*" #s ":" #r "+3+4*" #s "], v[32+8*" #s ":35+8*" #s "], " c0 "\n" \
    "v_mfma_f32_16x16x32_bf16 v[116:119], v[" #r "+4*" #s ":" #r "+3+4*" #s "], v[36+8*" #s ":39+8*" #s "], " c1 "\n"
#define D01 "v[112:115]", "v[116:119]"
#define GMF(r, s, n, c) GMF_(r, s, n, c)
#define DCVT "v_cvt_pk_bf16_f32 v112, v112, v113\n v_cvt_pk_bf16_f32 v113, v114, v115\n" \
             "v_cvt_pk_bf16_f32 v116, v116, v117\n v_cvt_pk_bf16_f32 v117, v118, v119\n"
#define V10(a) "v" STR(a) "0", "v" STR(a) "1", "v" STR(a) "2", "v" STR(a) "3", "v" STR(a) "4", "v" STR(a) "5", "v" STR(a) "6", "v" STR(a) "7", "v" STR(a) "8", "v" STR(a) "9"
#define CLOB_V "v25", "v26", "v27", "v28", "v29", V10(3), V10(4), V10(5), V10(6), V10(7), V10(8), V10(9), V10(10), V10(11), "v120", "v121", "v122", "v123"
#define CLOB_V2 "v25", "v26", "v27", "v28", "v29", V10(3), V10(4), V10(5), "v60", "v61", "v62", "v63", "v96", "v97", "v98", "v99", V10(10), V10(11), "v120", "v121", "v122", "v123"
#define CLOB_S "s40", "s41", "s42", "s43", "s44", "s45", "s46", "s47", "vcc", "scc", "memory"
#define AST(k)
#define CLOB_ST
#define STAMP_ARG

__global__ __launch_bounds__(1024) void featgraph_fused(const void* __restrict__ x, const int* __restrict__ src, const int* __restrict__ dst,
                                                       const void* __restrict__ wq, const void* __restrict__ wv,
                                                       char* __restrict__ ws, char* __restrict__ out STAMP_ARG) {
    __shared__ __attribute__((aligned(256))) char lds[L_END];
    int t = threadIdx.x;
    const int v = __builtin_amdgcn_readfirstlane(t >> 6);
    const int w = blockIdx.x, b = (w & 7) * 32 + (w >> 3);
    const i32x4 sx = make_srd(x, NN * 256), sw = make_srd(ws, WS_END);
    const unsigned d0 = 64 * ((t >> 5) & 1) + 16 * ((t & 3) ^ ((t >> 3) & 2));

    unsigned tag_x, tag_y;
    {
        const unsigned ea = t < CH / 4 ? (w * CH + t * 4) * 4 : OOB;
        const int r = t - 448;
        const unsigned wl0 = r < 128 ? (b * NPB * 2 + r) * 128 : r < 192 ? ((w >> 3) * 8 + (t & 7)) * 128 : r - 192 < NWARM ? ((w >> 3) * 784 + r - 192) * 128 : OOB;
        const unsigned wl1 = r + 384 < NWARM ? ((w >> 3) * 784 + r + 384) * 128 : OOB;
        const int u = v - 8, rho = (t >> 2) & 7;
        const unsigned xa = (b * NPB + 8 * u + rho) * 256 + d0;
        const unsigned wa = (32 * (u >> 1) + 4 * (u & 1) + 8 * (rho >> 2) + (rho & 3)) * 256 + d0;
        asm volatile(
            AST(0)
            "s_cmp_ge_u32 %[wv], 7\n s_cbranch_scc1 1f\n"
            "buffer_load_dwordx4 v[32:35], %[ea], %[rd], 0 offen\n"
            "buffer_load_dwordx4 v[36:39], %[ea], %[rs], 0 offen\n"
            "buffer_load_dwordx2 v[40:41], off, %[rw], 0 sc1\n"
            "s_branch 3f\n"
            "1:\n"
            "buffer_load_dwordx2 v[40:41], off, %[rw], 0 sc1\n"
            "s_cmp_eq_u32 %[wv], 9\n s_cbranch_scc1 2f\n"
            "buffer_load_dword v42, %[wl0], %[rx], 0 offen\n"
            "s_branch 3f\n"
            "2:\n"
            "buffer_load_dword v42, %[wl0], %[rq], 0 offen\n"
            "buffer_load_dword v43, %[wl0], %[rv], 0 offen\n"
            "3:\n"
            "v_lshlrev_b32 v24, 4, %[t]\n v_mov_b32 v28, 0xffffff\n v_mov_b32 v29, v28\n v_mov_b32 v30, v28\n v_mov_b32 v31, v28\n"
            "s_cmp_ge_u32 %[wv], 12\n s_cbranch_scc1 4f\n"
            "ds_write_b128 v24, v[28:31] offset:" STR(L_LIST_) "\n"
            "s_branch 5f\n"
            "4:\n"
            "v_and_b32 v25, 0xff, %[t]\n v_and_b32 v26, 15, %[t]\n v_mov_b32 v27, 0\n v_lshlrev_b32 v25, 2, v25\n"
            "v_mul_u32_u24 v28, " STR(CAPW_) "*4, v26\n v_lshlrev_b32 v26, 2, v26\n v_add_u32 v28, " STR(L_LIST_) ", v28\n"
            "ds_write_b32 v25, v27 offset:" STR(L_HIST_) "\n ds_write_b32 v26, v28\n"
            "5:\n"
            "v_mov_b32 v44, 4\n"
            "v_cmp_gt_u32 vcc, %[e4], %[ea]\n"
            "s_waitcnt lgkmcnt(0)\n s_barrier\n"
            AST(1)
            "s_cmp_ge_u32 %[wv], 7\n s_cbranch_scc1 6f\n"
            "s_mov_b64 exec, vcc\n"
            "s_waitcnt vmcnt(2)\n"
            ".irp j,0,1,2,3\n v_mul_u32_u24 v[48+\\j], 21400, v[32+\\j]\n .endr\n"
            ".irp j,0,1,2,3\n v_lshrrev_b32 v[48+\\j], 18, v[48+\\j]\n .endr\n"
            ".irp j,0,1,2,3\n v_and_b32 v[48+\\j], 0x3fc, v[48+\\j]\n ds_add_rtn_u32 v[52+\\j], v[48+\\j], v44 offset:" STR(L_HIST_) "\n .endr\n"
            ".irp j,0,1,2,3\n v_lshlrev_b32 v[32+\\j], 2, v[32+\\j]\n v_mad_i32_i24 v[32+\\j], v[48+\\j], %[m49], v[32+\\j]\n .endr\n"
            "s_waitcnt vmcnt(1)\n"
            ".irp j,0,1,2,3\n v_lshlrev_b32 v[36+\\j], 8, v[36+\\j]\n v_bfe_u32 v45, v[32+\\j], 6, 2\n v_and_or_b32 v[36+\\j], v[36+\\j], %[msk], v45\n"
            " v_lshl_or_b32 v[36+\\j], v[32+\\j], 26, v[36+\\j]\n .endr\n"
            "s_mov_b64 exec, -1\n"
            "6:\n"
            "v_and_b32 v25, 0x3f0, v24\n"
            "s_waitcnt lgkmcnt(0)\n s_barrier\n"
            AST(2)
            "s_cmp_ge_u32 %[wv], 8\n s_cbranch_scc1 7f\n"
            "ds_read_b128 v[28:31], v25 offset:" STR(L_HIST_) "\n"
            "s_waitcnt lgkmcnt(0)\n"
            "v_add_u32 v57, v28, v29\n v_add_u32 v58, v57, v30\n v_add_u32 v26, v58, v31\n s_nop 1\n"
            "v_add_u32_dpp v27, v26, v26 row_shr:1 row_mask:0xf bank_mask:0xf bound_ctrl:1\n s_nop 1\n"
            "v_add_u32_dpp v27, v27, v27 row_shr:2 row_mask:0xf bank_mask:0xf bound_ctrl:1\n s_nop 1\n"
            "v_add_u32_dpp v27, v27, v27 row_shr:4 row_mask:0xf bank_mask:0xf bound_ctrl:1\n s_nop 1\n"
            "v_add_u32_dpp v27, v27, v27 row_shr:8 row_mask:0xf bank_mask:0xf bound_ctrl:1\n s_nop 1\n"
            "v_add_u32_dpp v27, v27, v27 row_bcast:15 row_mask:0xa bank_mask:0xf\n s_nop 1\n"
            "v_add_u32_dpp v27, v27, v27 row_bcast:31 row_mask:0xc bank_mask:0xf\n"
            "v_sub_u32 v56, v27, v26\n v_add_u32 v59, v56, v58\n v_add_u32 v58, v56, v57\n v_add_u32 v57, v56, v28\n"
            "s_cmp_eq_u32 %[wv], 7\n s_cbranch_scc1 7f\n"
            "ds_write_b128 v25, v[56:59] offset:" STR(L_OFFS_) "\n"
            "s_mov_b64 exec, vcc\n"
            ".irp j,0,1,2,3\n ds_read_b32 v[48+\\j], v[48+\\j] offset:" STR(L_OFFS_) "\n .endr\n"
            "s_waitcnt lgkmcnt(0)\n"
            ".irp j,0,1,2,3\n v_add_u32 v[48+\\j], v[48+\\j], v[52+\\j]\n ds_write_b32 v[48+\\j], v[36+\\j] offset:" STR(L_SORT_) "\n .endr\n"
            "s_mov_b64 exec, -1\n"
            "7:\n"
            "s_waitcnt lgkmcnt(0)\n s_barrier\n"
            AST(3)
            "s_cmp_ge_u32 %[wv], 8\n s_cbranch_scc1 8f\n"
            "ds_read_b128 v[32:35], v24 offset:" STR(L_SORT_) "\n"
            "v_bfrev_b32 v26, 1\n v_add_u32 v27, " STR(WS_ENT_) ", %[ea]\n v_cndmask_b32 v26, v26, v27, vcc\n"
            "s_waitcnt lgkmcnt(0)\n"
            "buffer_store_dwordx4 v[32:35], v26, %[rw], 0 offen sc1\n"
            "s_branch 9f\n"
            "8:\n"
            "s_mov_b32 m0, %[mx]\n s_nop 0\n" DMA_BLK("%[xa]")
            "s_mov_b32 m0, %[mw]\n s_nop 0\n" DMA2("%[wa]", "%[rq]", "0", "%[c128]") "s_nop 0\n" DMA2("%[wa]", "%[rq]", "%[c4k]", "%[c4k2]")
            "s_add_u32 m0, m0, 0x7000\n s_nop 0\n" DMA2("%[wa]", "%[rv]", "0", "%[c128]") "s_nop 0\n" DMA2("%[wa]", "%[rv]", "%[c4k]", "%[c4k2]")
            "9:\n"
            "s_waitcnt vmcnt(0)\n"
            "v_readfirstlane_b32 %[tx], v40\n v_readfirstlane_b32 %[ty], v41\n"
            AST(4)
            "s_add_u32 %[tx], %[tx], 1\n s_addc_u32 %[ty], %[ty], 0\n"
            "s_cmp_lg_u32 %[wv], 7\n s_cbranch_scc1 10f\n"
            "s_lshl_b32 s40, %[tx], 22\n s_lshr_b32 s41, %[tx], 10\n s_lshl_b32 s42, %[ty], 22\n s_or_b32 s41, s41, s42\n"
            ".irp j,0,1,2,3\n v_lshrrev_b32 v[32+2*\\j], 2, v[56+\\j]\n v_lshl_or_b32 v[32+2*\\j], v[28+\\j], 9, v[32+2*\\j]\n"
            " v_or_b32 v[32+2*\\j], s40, v[32+2*\\j]\n v_mov_b32 v[33+2*\\j], s41\n .endr\n"
            "v_lshlrev_b32 v25, 1, v25\n"
            "s_barrier\n"
            "buffer_store_dwordx4 v[32:35], v25, %[rw], %[to] offen sc1\n"
            "buffer_store_dwordx4 v[36:39], v25, %[rw], %[to] offen offset:16 sc1\n"
            "s_nop 1\n"
            "s_branch 11f\n"
            "10:\n"
            "s_barrier\n"
            "11:\n"
            : [tx] "=&s"(tag_x), [ty] "=&s"(tag_y)
            : [t] "v"(t), [ea] "v"(ea), [wl0] "v"(wl0), [wl1] "v"(wl1), [xa] "v"(xa), [wa] "v"(wa), [wv] "s"(v),
              [rx] "s"(sx), [rw] "s"(sw), [rd] "s"(make_srd(dst, EE * 4)), [rs] "s"(make_srd(src, EE * 4)),
              [rq] "s"(make_srd(wq, 32768)), [rv] "s"(make_srd(wv, 32768)), [e4] "s"(EE * 4), [m49] "s"(-NPB), [msk] "s"(0x00ffff00),
              [c128] "s"(128), [c4k] "s"(4096), [c4k2] "s"(4224), [mx] "s"(L_X + 2048 * u), [mw] "s"(L_WQ + 4096 * u),
              [to] "s"(WS_TAB + w * NB * 8)
            : "v24", "v25", "v26", "v27", "v28", "v29", V10(3), V10(4), V10(5), CLOB_S CLOB_ST);
    }
    const unsigned tag_lo = tag_x << 22, tag_hi = tag_x >> 10 | tag_y << 22;

    asm volatile("" : "+v"(t));
    const int lane = t & 63, g = lane >> 4, m = t & 15, nt = v & 7, mh = v >> 3;
    const unsigned lbase = L_LIST + v * CAPW * 4;
    const unsigned buf = L_X + v * 8192;
    const unsigned trl = 2048 * (g >> 1) + 256 * (g & 1) + 64 * (m >> 2) + 16 * (((t >> 1) & 1) ^ (2 * (g & 1))) + 8 * (t & 1);
    const unsigned srl = 2048 * (m >> 3) + 64 * (m & 7) + 16 * (g ^ ((m >> 1) & 2));
    const unsigned lp = lbase + ((t >> 2) & 7) * 4;
    unsigned cnt_l, cnt_v;
    u32x4 wf0, wf1, wf2, wf3;
    {
        const unsigned ei = (t & 3) * 4;
        asm volatile(
            "buffer_load_dwordx2 v[24:25], %[pa], %[rw], 0 offen sc1\n"
            WTR_(96, 0, 0) BRD_(0, 512, 4096) WTR_(96, 1, 0) BRD_(1, 512, 4096) WTR_(96, 2, 0) BRD_(2, 512, 4096) WTR_(96, 3, 0) BRD_(3, 512, 4096)
            GMF_(96, 0, 12, "0", "0") GMF(96, 1, 8, D01) GMF(96, 2, 4, D01) GMF(96, 3, 0, D01)
            WTR_(8, 0, 32768) WTR_(8, 1, 32768) WTR_(8, 2, 32768) WTR_(8, 3, 32768)
            DCVT
            "ds_write_b64 %[qw], v[112:113]\n ds_write_b64 %[qw], v[116:117] offset:" STR(16*288+16) "\n"
            "v_mov_b32 v26, 4\n v_bfrev_b32 v27, 1\n"
            "s_mov_b32 s40, 0x20000\n"
            AST(5)
            "1:\n"
            "s_waitcnt vmcnt(0)\n"
            "v_xor_b32 v24, %[tlo], v24\n"
            "v_cmp_ne_u32_e64 s[42:43], %[thi], v25\n"
            "v_cmp_le_u32 vcc, 0x400000, v24\n"
            "s_or_b64 exec, vcc, s[42:43]\n"
            "s_cbranch_execz 2f\n"
            "s_sub_u32 s40, s40, 1\n"
            "s_cbranch_scc1 2f\n"
            "s_sleep 1\n"
            "buffer_load_dwordx2 v[24:25], %[pa], %[rw], 0 offen sc1\n"
            "s_branch 1b\n"
            "2:\n"
            "s_mov_b64 exec, -1\n"
            AST(6)
            "v_bfe_u32 v25, v24, 0, 11\n"
            "v_bfe_u32 v24, v24, 11, 11\n"
            "v_lshl_add_u32 v25, v25, 2, %[eb]\n"
            "v_sub_u32 v24, v24, %[ei]\n"
            "4:\n"
            "v_cmp_lt_i32_e64 s[40:41], 0, v24\n v_cmp_lt_i32_e64 s[42:43], 1, v24\n"
            "v_cmp_lt_i32_e64 s[44:45], 2, v24\n v_cmp_lt_i32_e64 s[46:47], 3, v24\n"
            "v_cndmask_b32_e64 v28, v27, v25, s[40:41]\n"
            "buffer_load_dwordx4 v[28:31], v28, %[rw], 0 offen sc1\n"
            "v_cmp_lt_i32 vcc, 16, v24\n"
            "s_waitcnt vmcnt(0)\n"
            AST(7)
            "v_lshrrev_b32 v120, 26, v28\n v_lshrrev_b32 v121, 26, v29\n v_lshrrev_b32 v122, 26, v30\n v_lshrrev_b32 v123, 26, v31\n"
            "s_mov_b64 exec, s[40:41]\n ds_add_rtn_u32 v120, v120, v26\n"
            "s_mov_b64 exec, s[42:43]\n ds_add_rtn_u32 v121, v121, v26\n"
            "s_mov_b64 exec, s[44:45]\n ds_add_rtn_u32 v122, v122, v26\n"
            "s_mov_b64 exec, s[46:47]\n ds_add_rtn_u32 v123, v123, v26\n"
            "s_mov_b64 exec, -1\n"
            "s_waitcnt lgkmcnt(0)\n"
            "v_min_u32 v120, %[lmax], v120\n v_min_u32 v121, %[lmax], v121\n v_min_u32 v122, %[lmax], v122\n v_min_u32 v123, %[lmax], v123\n"
            "s_mov_b64 exec, s[40:41]\n ds_write_b32 v120, v28\n"
            "s_mov_b64 exec, s[42:43]\n ds_write_b32 v121, v29\n"
            "s_mov_b64 exec, s[44:45]\n ds_write_b32 v122, v30\n"
            "s_mov_b64 exec, s[46:47]\n ds_write_b32 v123, v31\n"
            "s_mov_b64 exec, -1\n"
            "s_waitcnt lgkmcnt(0)\n"
            "s_cbranch_vccz 5f\n"
            "v_add_u32 v24, -16, v24\n v_add_u32 v25, 64, v25\n"
            "s_branch 4b\n"
            "5:\n"
            AST(8)
            "s_barrier\n"
            "ds_read_b32 v24, %[ca]\n"
            "ds_read2_b32 v[28:29], %[lp] offset0:0 offset1:8\n"
            "ds_read2_b32 v[30:31], %[lp] offset0:16 offset1:24\n"
            "s_mov_b32 m0, %[buf]\n"
            "s_waitcnt lgkmcnt(2)\n"
            "v_readlane_b32 %[cnt], v24, %[wv]\n"
            "s_waitcnt lgkmcnt(0)\n"
            "v_and_or_b32 v28, v28, %[msk], %[d0]\n v_and_or_b32 v29, v29, %[msk], %[d0]\n"
            "v_and_or_b32 v30, v30, %[msk], %[d0]\n v_and_or_b32 v31, v31, %[msk], %[d0]\n"
            "s_sub_i32 %[cnt], %[cnt], %[lb4]\n"
            "s_max_i32 %[cnt], %[cnt], 0\n"
            "s_lshr_b32 s40, %[cnt], 7\n"
            PRIO("s40")
            DMA_BLK("v28") "s_nop 0\n" DMA_BLK("v29") "s_nop 0\n" DMA_BLK("v30") "s_nop 0\n" DMA_BLK("v31")
            AST(9)
            : "=&{v24}"(cnt_l), [cnt] "=&s"(cnt_v), "=&{v[8:11]}"(wf0), "=&{v[12:15]}"(wf1), "=&{v[16:19]}"(wf2), "=&{v[20:23]}"(wf3)
            : [pa] "v"(WS_TAB + ((t >> 2) * NB + b) * 8), [ei] "v"(ei), [eb] "v"(WS_ENT + ((t >> 2) * CH + ei) * 4),
              [gb] "v"(L_X + 8192 * mh + srl), [tq] "v"(L_WQ + (trl ^ (32 * (nt & 1))) + 512 * (nt >> 1)),
              [qw] "v"(L_QS + 2 * mh * QT + m * QP + nt * 32 + g * 8),
              [lp] "v"(lp), [ca] "v"(L_CNT + m * 4), [d0] "v"(d0), [wv] "s"(v), [lb4] "s"(lbase + 4), [tlo] "s"(tag_lo), [thi] "s"(tag_hi),
              [rw] "s"(sw), [rx] "s"(sx), [c128] "s"(128), [msk] "s"(0x00ffff00), [buf] "s"(buf), [lmax] "s"(L_QS - 4)
            : CLOB_V2, CLOB_S CLOB_ST);
        cnt_l -= L_LIST + m * CAPW * 4;
        cnt_v = __builtin_amdgcn_readfirstlane(cnt_v);
    }
    auto rx = __builtin_amdgcn_make_buffer_rsrc((void*)x, 0, NN * 256, 0x00020000);
    auto rw = __builtin_amdgcn_make_buffer_rsrc((void*)ws, 0, WS_END, 0x00020000);
    if (w == 0 && t == 0) __builtin_amdgcn_raw_buffer_store_b64(u32x2{tag_x, tag_y}, rw, WS_EPOCH, 0, SC1);
    const unsigned qrow = L_QS + v * QP + (m >> 2) * QT;
    if (__ballot(cnt_l > CAPW * 4) == 0) {
        const int bq = t & 3;
        const unsigned np = lbase + (16 * (bq >> 1) + 4 * g + 2 * (bq & 1)) * 4;
        unsigned left = cnt_v >> 7, cur = 0, nxt = cnt_v & ~127u, seq = 128, rows = cnt_v & 127;
        asm volatile(
            "ds_read_b128 v[96:99], %[qa]\n ds_read_b128 v[100:103], %[qa] offset:64\n"
            "ds_read_b128 v[104:107], %[qa] offset:128\n ds_read_b128 v[108:111], %[qa] offset:192\n"
            ".irp i,64,65,66,67,68,69,70,71,72,73,74,75,76,77,78,79,80,81,82,83,84,85,86,87,88,89,90,91,92,93,94,95\n v_mov_b32 v\\i, 0\n .endr\n"
            "s_mov_b32 s40, 0xaaaaaaaa\n s_mov_b32 s41, s40\n s_mov_b32 s42, 0xcccccccc\n s_mov_b32 s43, s42\n"
            "s_sub_i32 s44, %[left], 1\n"
            PRIO("s44")
            "1:\n"
            "v_add_u32 v30, %[cur], %[np]\n v_add_u32 v31, %[nxt], %[lp]\n"
            "ds_read_b64 v[28:29], v30\n"
            "ds_read2_b32 v[24:25], v31 offset0:0 offset1:8\n"
            "ds_read2_b32 v[26:27], v31 offset0:16 offset1:24\n"
            "s_mov_b32 %[cur], %[nxt]\n s_mov_b32 %[nxt], %[seq]\n s_add_u32 %[seq], %[seq], 0x80\n"
            "s_waitcnt vmcnt(0)\n"
            SRD_(0) SRD_(1) SRD_(2) SRD_(3) SRD_(4) SRD_(5) SRD_(6) SRD_(7)
            SMF_(0, 7, "v[112:115]", "0") TRD_(0, "t0")
            SMF_(1, 8, "v[116:119]", "0") TRD_(1, "t1")
            SMF_(2, 9, "v[112:115]", "v[112:115]") TRD_(2, "t0")
            SMF_(3, 10, "v[116:119]", "v[116:119]") TRD_(3, "t1")
            SMF_(4, 11, "v[112:115]", "v[112:115]") TRD_(4, "t0")
            SMF_(5, 12, "v[116:119]", "v[116:119]") TRD_(5, "t1")
            SMF_(6, 13, "v[112:115]", "v[112:115]") TRD_(6, "t0")
            SMF_(7, 14, "v[116:119]", "v[116:119]") TRD_(7, "t1")
            "v_and_or_b32 v24, v24, %[msk], %[d0]\n v_and_or_b32 v25, v25, %[msk], %[d0]\n"
            "v_and_or_b32 v26, v26, %[msk], %[d0]\n v_and_or_b32 v27, v27, %[msk], %[d0]\n"
            "v_cmp_eq_u32_sdwa s[44:45], v28, %[av] src0_sel:BYTE_0 src1_sel:DWORD\n"
            "v_cmp_eq_u32_sdwa s[46:47], v29, %[av] src0_sel:BYTE_0 src1_sel:DWORD\n"
            "v_cndmask_b32_e64 v28, v112, v114, s[40:41]\n v_cndmask_b32_e64 v29, v113, v115, s[40:41]\n"
            "v_cndmask_b32_e64 v30, v116, v118, s[40:41]\n v_cndmask_b32_e64 v31, v117, v119, s[40:41]\n"
            "v_cndmask_b32_e64 v28, v28, v30, s[42:43]\n v_cndmask_b32_e64 v29, v29, v31, s[42:43]\n"
            "v_mul_f32 v28, 0xbe0293ee, v28\n v_mul_f32 v29, 0xbe0293ee, v29\n"
            "v_exp_f32 v28, v28\n v_exp_f32 v29, v29\n"
            "s_mov_b32 m0, %[buf]\n"
            "s_cmp_eq_u32 %[left], 0\n"
            "s_waitcnt lgkmcnt(0)\n"
            "s_cbranch_scc1 2f\n"
            DMA_BLK("v24") "s_cmp_lt_u32 %[rows], 32\n s_cbranch_scc1 9f\n"
            DMA_BLK("v25") "s_cmp_lt_u32 %[rows], 64\n s_cbranch_scc1 9f\n"
            DMA_BLK("v26") "s_cmp_lt_u32 %[rows], 96\n s_cbranch_scc1 9f\n"
            DMA_BLK("v27")
            "9:\n"
            "s_movk_i32 %[rows], 0x7f\n"
            "s_sub_i32 vcc_lo, %[left], 2\n"
            PRIO("vcc_lo")
            "2:\n"
            "v_add_f32 v28, 1.0, v28\n v_add_f32 v29, 1.0, v29\n"
            "v_rcp_f32 v28, v28\n v_rcp_f32 v29, v29\n"
            "s_nop 0\n"
            "v_cndmask_b32_e64 v28, 0, v28, s[44:45]\n v_cndmask_b32_e64 v29, 0, v29, s[46:47]\n"
            "v_cvt_pk_bf16_f32 v28, v28, v29\n"
            "s_cmp_lg_u32 %[left], 0\n s_nop 0\n"
            "v_mov_b32_dpp v120, v28 quad_perm:[0,0,0,0] row_mask:0xf bank_mask:0xf\n"
            "v_mov_b32_dpp v121, v28 quad_perm:[1,1,1,1] row_mask:0xf bank_mask:0xf\n"
            "v_mov_b32_dpp v122, v28 quad_perm:[2,2,2,2] row_mask:0xf bank_mask:0xf\n"
            "v_mov_b32_dpp v123, v28 quad_perm:[3,3,3,3] row_mask:0xf bank_mask:0xf\n"
            "s_nop 1\n"
            PMF_(0) PMF_(1) PMF_(2) PMF_(3) PMF_(4) PMF_(5) PMF_(6) PMF_(7)
            "s_cbranch_scc0 4f\n"
            "s_sub_u32 %[left], %[left], 1\n"
            "s_branch 1b\n"
            "4:\n"
            "s_nop 1\n"
            CVT_(0) CVT_(1) CVT_(2) CVT_(3) CVT_(4) CVT_(5) CVT_(6) CVT_(7) SEL_(0) SEL_(1) SEL_(2) SEL_(3)
            AST(10)
            "s_barrier\n"
            "ds_write_b64 %[fa], v[32:33]\n ds_write_b64 %[fa], v[34:35] offset:32\n"
            : [left] "+&s"(left), [cur] "+&s"(cur), [nxt] "+&s"(nxt), [seq] "+&s"(seq), [rows] "+&s"(rows)
            : [lp] "v"(lp), [np] "v"(np), [d0] "v"(d0), [s0] "v"(buf + srl), [t0] "v"(buf + trl), [t1] "v"(buf + (trl ^ 32)), [av] "v"(m >> 2),
              [qa] "v"(qrow + 16 * g), [fa] "v"(qrow + 8 * g + 64 * bq),
              [buf] "s"(buf), [rx] "s"(sx), [c128] "s"(128), [msk] "s"(0x00ffff00)
            : "v24", CLOB_V, CLOB_S CLOB_ST);
    } else {
        float ac[4][2] = {};
        for (int c = 0; c < NB; c++) {
            const unsigned tw = __builtin_amdgcn_readfirstlane(__builtin_amdgcn_raw_buffer_load_b32(rw, WS_TAB + (c * NB + b) * 8, 0, SC1));
            const unsigned sg = WS_ENT + (c * CH + (tw & 0x7ffu)) * 4;
            for (int i = 0; i < (int)(tw >> 11 & 0x7ffu); i++) {
                const unsigned e = __builtin_amdgcn_readfirstlane(__builtin_amdgcn_raw_buffer_load_b32(rw, sg + i * 4, 0, SC1));
                if ((int)(e >> 28) != v) continue;
                const unsigned xv = __builtin_amdgcn_raw_buffer_load_b32(rx, (e & 0xffff00u) + lane * 4, 0, 0);
                const unsigned qv = *(const unsigned*)(lds + L_QS + v * QP + (e & 3) * QT + lane * 4);
                const float x0 = __builtin_bit_cast(float, xv << 16), x1 = __builtin_bit_cast(float, xv & 0xffff0000u);
                float s = x0 * __builtin_bit_cast(float, qv << 16) + x1 * __builtin_bit_cast(float, qv & 0xffff0000u);
#pragma unroll
                for (int o = 32; o > 0; o >>= 1) s += __shfl_xor(s, o, 64);
                const float al = (float)(__bf16)__builtin_amdgcn_rcpf(1.0f + __builtin_amdgcn_exp2f(s * -0.12751743f));
#pragma unroll
                for (int a = 0; a < 4; a++) {
                    const float aa = (int)(e & 3) == a ? al : 0.f;
                    ac[a][0] += aa * x0;
                    ac[a][1] += aa * x1;
                }
            }
        }
        asm volatile("s_waitcnt vmcnt(0)\n s_barrier" ::: "memory");
#pragma unroll
        for (int a = 0; a < 4; a++) *(unsigned*)(lds + L_QS + v * QP + a * QT + lane * 4) = pk_bf16(ac[a][0], ac[a][1]);
    }
    {
        asm volatile("" : "+v"(t));
        const int g2 = (t >> 4) & 3, m2 = t & 15;
        const unsigned prow = 2 * mh * QT + m2 * QP, node = t >> 4;
        asm volatile(
            "s_waitcnt lgkmcnt(0)\n"
            "s_barrier\n"
            AST(11)
            BRD_(0, 64, 16*288+16) BRD_(1, 64, 16*288+16) BRD_(2, 64, 16*288+16) BRD_(3, 64, 16*288+16)
            GMF_(8, 0, 6, "0", "0") GMF(8, 1, 4, D01) GMF(8, 2, 2, D01) GMF(8, 3, 0, D01)
            "s_nop 6\n"
            DCVT
            "ds_write_b64 %[ow], v[112:113]\n ds_write_b64 %[ow], v[116:117] offset:" STR(16*288+16) "\n"
            "s_waitcnt lgkmcnt(0)\n"
            AST(12)
            "s_barrier\n"
            "ds_read_b128 v[28:31], %[orr]\n"
            "s_waitcnt lgkmcnt(0)\n"
            "buffer_store_dwordx4 v[28:31], %[oa], %[ro], 0 offen" OUTPOL "\n"
            AST(13)
            :: [gb] "v"(L_QS + prow + g2 * 16), [ow] "v"(L_OUT + prow + nt * 32 + g2 * 8),
               [orr] "v"(L_OUT + (node >> 4) * QT + (node & 15) * QP + 16 * m2), [oa] "v"(node < NPB ? b * NPB * 256 + t * 16 : OOB),
               [ro] "s"(make_srd(out, NN * 256)), "{v[8:11]}"(wf0), "{v[12:15]}"(wf1), "{v[16:19]}"(wf2), "{v[20:23]}"(wf3)
            : "v24", CLOB_V2, CLOB_S CLOB_ST);
    }
}

#define ST1

extern "C" void kernel_launch(void* const* d_in, const int* in_sizes, int n_in, void* d_out, int out_size,
                              void* d_ws, size_t ws_size, hipStream_t stream) {
    featgraph_fused<<<NB, 1024, 0, stream>>>(d_in[0], (const int*)d_in[1], (const int*)d_in[2], d_in[3], d_in[4],
                                             (char*)d_ws, (char*)d_out ST1);
}
```

```cpp
#include <hip/hip_runtime.h>
#include <hip/hip_bf16.h>

typedef unsigned u32x4 __attribute__((ext_vector_type(4)));
typedef unsigned u32x2 __attribute__((ext_vector_type(2)));
typedef int i32x4 __attribute__((ext_vector_type(4)));
typedef __bf16 bf16x2 __attribute__((ext_vector_type(2)));

constexpr int NN = 12500, EE = 400000;
constexpr int NB = 256;
constexpr int NC = 128;
constexpr int NPB = 49;
constexpr int CH = 3128;
constexpr int CHP = 3136;
constexpr int CAPW = 192;
constexpr unsigned OOB = 0x80000000u;
constexpr unsigned SENT = 0x00ffffffu;
constexpr unsigned TAGM = 0xffc00003u;
constexpr int SC1 = 16;

constexpr unsigned WS_EPOCH = 0;
constexpr unsigned WS_ACK = 256;
constexpr unsigned WS_TAB = 1280;
constexpr unsigned WS_ENT = WS_TAB + NC * NB * 8;
constexpr unsigned WS_END = WS_ENT + NC * CHP * 4 + 256;

#define NWARM 512

__device__ __forceinline__ unsigned pk_bf16(float a, float b) {
    bf16x2 v = {(__bf16)a, (__bf16)b};
    return __builtin_bit_cast(unsigned, v);
}
__device__ __forceinline__ i32x4 make_srd(const void* p, unsigned bytes) {
    const unsigned long long a = (unsigned long long)p;
    return i32x4{(int)a, (int)(a >> 32) & 0xffff, (int)bytes, 0x00020000};
}

constexpr int QP = 288, QT = 16 * QP + 16;
constexpr int L_CNT = 0;
constexpr int L_LIST = 64;
constexpr int L_QS = L_LIST + 16 * CAPW * 4;
constexpr int L_X = (L_QS + 4 * QT + 255) / 256 * 256;
constexpr int L_WQ = L_X + 16384, L_WV = L_WQ + 32768;
constexpr int L_OUT = L_X;
constexpr int L_END = L_X + 16 * 8192;
#define CAPW_ 192
#define L_LIST_ 64
#define L_HIST_ 12352
#define L_OFFS_ 13376
#define L_SORT_ 14400
#define WS_ENT_ 263424
static_assert(CAPW_ == CAPW && L_LIST_ == L_LIST && L_HIST_ == L_QS && WS_ENT_ == WS_ENT && L_SORT_ + CH * 4 + 1024 <= L_X, "asm literals");

#define PRIO(r) "s_setprio 0\n s_cmp_lt_i32 " r ", 1\n s_cbranch_scc1 8f\n s_setprio 1\n s_cmp_lt_i32 " r ", 2\n s_cbranch_scc1 8f\n" \
                "s_setprio 2\n s_cmp_lt_i32 " r ", 3\n s_cbranch_scc1 8f\n s_setprio 3\n 8:\n"
#define CPOS 1
#define VAR 0
#define NAFTER 3
#define GSLEEP 4
#define PSLEEP 2
#define OUTPOL " sc0 sc1 nt"
#define STR_(x) #x
#define STR(x) STR_(x)
#define DMA2(ent, srd, o0, o1) \
    "buffer_load_dwordx4 " ent ", " srd ", " o0 " offen lds\n" \
    "s_add_u32 m0, m0, 0x400\n s_nop 0\n" \
    "buffer_load_dwordx4 " ent ", " srd ", " o1 " offen lds\n" \
    "s_add_u32 m0, m0, 0x400\n"
#define DMA_BLK(ent) DMA2(ent, "%[rx]", "0", "%[c128]")
#define STAGE "s_mov_b32 m0, %[mx]\n s_nop 0\n buffer_load_dwordx4 %[xa], %[rx], 0 offen lds\n" \
              "s_mov_b32 m0, %[mw]\n s_nop 0\n" DMA2("%[wa]", "%[rq]", "0", "%[c128]")
#define TAGS "v_readfirstlane_b32 %[tx], v40\n v_readfirstlane_b32 %[ty], v41\n" \
             "s_add_u32 %[tx], %[tx], 1\n s_addc_u32 %[ty], %[ty], 0\n" \
             "s_lshl_b32 s54, %[tx], 24\n s_lshr_b32 s55, %[tx], 8\n s_lshl_b32 s56, %[ty], 24\n s_or_b32 s55, s55, s56\n" \
             "s_lshl_b32 s56, %[tx], 22\n s_bfe_u32 s57, %[tx], 0x2000a\n s_or_b32 s56, s56, s57\n s_xor_b32 s57, s56, 0xffc00003\n"
#define POLL "buffer_load_dwordx2 v[88:89], %[pa], %[rw], 0 offen sc1\n"
#define SRD_(i) "ds_read_b128 v[32+4*" #i ":35+4*" #i "], %[s0] offset:512*(" #i "/2)+4096*(" #i "&1)\n"
#define TRD_(i, a) "ds_read_b64_tr_b16 v[32+4*" #i ":33+4*" #i "], %[" a "] offset:512*(" #i "/2)\n" \
                   "ds_read_b64_tr_b16 v[34+4*" #i ":35+4*" #i "], %[" a "] offset:512*(" #i "/2)+4096\n"
#define SMF_(i, n, d, c) "s_waitcnt lgkmcnt(" #n ")\n v_mfma_f32_16x16x32_bf16 " d ", v[32+4*" #i ":35+4*" #i "], v[96+4*(" #i "/2):99+4*(" #i "/2)], " c "\n"
#define PMF_(i) "v_mfma_f32_16x16x32_bf16 v[64+4*" #i ":67+4*" #i "], v[32+4*" #i ":35+4*" #i "], v[120:123], v[64+4*" #i ":67+4*" #i "]\n"
#define CVT_(i) "v_cvt_pk_bf16_f32 v[32+2*" #i "], v[64+4*" #i "], v[65+4*" #i "]\n v_cvt_pk_bf16_f32 v[33+2*" #i "], v[66+4*" #i "], v[67+4*" #i "]\n"
#define SEL_(i) "v_cndmask_b32_e64 v[32+" #i "], v[32+" #i "], v[36+" #i "], s[40:41]\n v_cndmask_b32_e64 v[40+" #i "], v[40+" #i "], v[44+" #i "], s[40:41]\n" \
                "v_cndmask_b32_e64 v[32+" #i "], v[32+" #i "], v[40+" #i "], s[42:43]\n"
#define WTR_(r, s, a, o, h) "ds_read_b64_tr_b16 v[" #r "+4*" #s ":" #r "+1+4*" #s "], %[" a "] offset:" #o "\n" \
                            "ds_read_b64_tr_b16 v[" #r "+2+4*" #s ":" #r "+3+4*" #s "], %[" a "] offset:" #o "+" #h "\n"
#define BRD_(s, a, ks, km) "ds_read_b128 v[32+8*" #s ":35+8*" #s "], %[" a "] offset:" #ks "*" #s "\n" \
                           "ds_read_b128 v[36+8*" #s ":39+8*" #s "], %[" a "] offset:" #ks "*" #s "+" #km "\n"
#define GMF_(r, s, n, c0, c1) "s_waitcnt lgkmcnt(" #n ")\n" \
    "v_mfma_f32_16x16x32_bf16 v[112:115], v[" #r "+4*" #s ":" #r "+3+4*" #s "], v[32+8*" #s ":35+8*" #s "], " c0 "\n" \
    "v_mfma_f32_16x16x32_bf16 v[116:119], v[" #r "+4*" #s ":" #r "+3+4*" #s "], v[36+8*" #s ":39+8*" #s "], " c1 "\n"
#define D01 "v[112:115]", "v[116:119]"
#define GMF(r, s, n, c) GMF_(r, s, n, c)
#define DCVT "v_cvt_pk_bf16_f32 v112, v112, v113\n v_cvt_pk_bf16_f32 v113, v114, v115\n" \
             "v_cvt_pk_bf16_f32 v116, v116, v117\n v_cvt_pk_bf16_f32 v117, v118, v119\n"
#define V10(a) "v" STR(a) "0", "v" STR(a) "1", "v" STR(a) "2", "v" STR(a) "3", "v" STR(a) "4", "v" STR(a) "5", "v" STR(a) "6", "v" STR(a) "7", "v" STR(a) "8", "v" STR(a) "9"
#define CLOB_V "v25", "v26", "v27", "v28", "v29", V10(3), V10(4), V10(5), V10(6), V10(7), V10(8), V10(9), V10(10), V10(11), "v120", "v121", "v122", "v123"
#define CLOB_V2 "v25", "v26", "v27", "v28", "v29", V10(3), V10(4), V10(5), "v60", "v61", "v62", "v63", "v96", "v97", "v98", "v99", V10(10), V10(11), "v120", "v121", "v122", "v123"
#define CLOB_S "s40", "s41", "s42", "s43", "s44", "s45", "s46", "s47", "vcc", "scc", "memory"
#define AST_INIT
#define AST(k)
#define CLOB_ST
#define STAMP_ARG

__global__ __launch_bounds__(1024) void featgraph_fused(const void* __restrict__ x, const int* __restrict__ src, const int* __restrict__ dst,
                                                       const void* __restrict__ wq, const void* __restrict__ wv,
                                                       char* __restrict__ ws, char* __restrict__ out STAMP_ARG) {
    __shared__ __attribute__((aligned(256))) char lds[L_END];
    int t = threadIdx.x;
    const int w = blockIdx.x, c = (w >> 4) * 8 + (w & 7);
    const bool sorter = !(w & 8);
    const unsigned ea = sorter && t < CH / 4 && c * CH + t * 4 < EE ? (c * CH + t * 4) * 4 : OOB;
    u32x4 d4, s4;
    asm volatile(AST_INIT AST(0)
                 "v_cmp_le_i32 vcc, 0, %[ea]\n s_and_b64 exec, exec, vcc\n s_cbranch_execz 1f\n"
                 "global_load_dwordx4 v[32:35], %[ea], %[pd]\n global_load_dwordx4 v[36:39], %[ea], %[ps]\n"
                 "1:\n s_mov_b64 exec, -1\n"
                 : "=&{v[32:35]}"(d4), "=&{v[36:39]}"(s4) : [ea] "v"(ea), [pd] "s"(dst), [ps] "s"(src) : "vcc", "scc", "memory" CLOB_ST);
    asm volatile("" : "+v"(t));
    const int v = __builtin_amdgcn_readfirstlane(t >> 6);
    const int b = (w & 7) * 32 + (w >> 3);
    const i32x4 sx = make_srd(x, NN * 256), sw = make_srd(ws, WS_END), sq = make_srd(wq, 32768), sv = make_srd(wv, 32768);
    const int rho = (t >> 2) & 7;
    const unsigned d0 = 64 * ((t >> 5) & 1) + 16 * ((t & 3) ^ ((t >> 3) & 2));
    const unsigned xa = (b * NPB + 8 * (v >> 1) + rho) * 256 + d0 + 128 * (v & 1);
    const unsigned wa = (32 * (v >> 2) + 16 * (v & 1) + 4 * ((v >> 1) & 1) + 8 * (rho >> 2) + (rho & 3)) * 256 + d0;
    u32x2 ep;
    unsigned wm0, wm1, wm2;
    asm volatile(
        "buffer_load_dwordx2 v[40:41], off, %[rw], 0 sc1\n"
        "s_cmp_eq_u32 %[srt], 0\n s_cbranch_scc0 3f\n"
        "s_cmp_lg_u32 %[wv], 0\n s_cbranch_scc1 2f\n"
        "buffer_load_dword v85, %[pf], %[rq], 0 offen\n buffer_load_dword v86, %[pf], %[rv], 0 offen\n"
        "2:\n"
        STAGE
        "s_barrier\n"
        "s_cmp_ge_u32 %[wv], (" STR(NWARM) "+63)/64\n s_cbranch_scc1 3f\n"
        "buffer_load_dword v84, %[wl], %[rx], 0 offen\n"
        "3:\n"
        : "=&{v[40:41]}"(ep), "=&{v84}"(wm0), "=&{v85}"(wm1), "=&{v86}"(wm2)
        : [xa] "v"(xa), [wa] "v"(wa), [wl] "v"(((w >> 4) * 1568 + t) * 128),
          [pf] "v"(((w >> 4) * 16 + (t & 15)) * 128), [wv] "s"(v), [srt] "s"((int)sorter),
          [rx] "s"(sx), [rw] "s"(sw), [rq] "s"(sq), [rv] "s"(sv), [c128] "s"(128), [mx] "s"(L_X + 1024 * v), [mw] "s"(L_WQ + 2048 * v)
        : "scc", "memory");
    if (v < 12) *(u32x4*)(lds + L_LIST + t * 16) = u32x4{SENT, SENT, SENT, SENT};
    else {
        *(unsigned*)(lds + L_HIST_ + (t & 255) * 4) = 0;
        *(unsigned*)(lds + L_CNT + (t & 15) * 4) = L_LIST + (t & 15) * CAPW * 4;
    }
    const unsigned tcol = (b >> 1 & 1) * 1024 + (b >> 2) * 16 + (b & 1) * 8;
    const unsigned t16 = t * 16, eo = t < CHP / 4 ? WS_ENT + c * CHP * 4 + t16 : OOB, pa = WS_TAB + (t >> 3) * NB * 8 + tcol;
    u32x2 tw;
    asm volatile("" : "+v"(t));
    const int lane = t & 63, g = lane >> 4, m = t & 15, bq = t & 3, nt = v & 7, mh = v >> 3;
    const unsigned lbase = L_LIST + v * CAPW * 4, buf = L_X + v * 8192;
    const unsigned trl = 256 * (g & 1) + 64 * (m >> 2) + 16 * (((t >> 1) & 1) ^ (2 * (g & 1))) + 8 * (t & 1);
    const unsigned trn = (trl ^ (32 * (nt & 1))) + 512 * (nt >> 1);
    const unsigned srl = 2048 * (m >> 3) + 64 * (m & 7) + 16 * (g ^ ((m >> 1) & 2));
    const unsigned prow = 2 * mh * QT + m * QP, lp = lbase + ((t >> 2) & 7) * 4;
    unsigned j4 = (t & 7) * 4, eb = WS_ENT + (t >> 3) * CHP * 4 + (t & 7) * 16, gb = L_X + 8192 * mh + srl, tq = L_WQ + 2048 * (g >> 1) + trn;
    unsigned qw = L_QS + prow + nt * 32 + g * 8, ca = L_CNT + m * 4, lim = L_LIST + (m + 1) * CAPW * 4, lpp = lp;
    asm volatile("" : "+v"(j4), "+v"(eb), "+v"(gb), "+v"(tq), "+v"(qw), "+v"(ca), "+v"(lim), "+v"(lpp));
    unsigned tag_x, tag_y, tag_lo, tag_hi, tag_e;
    asm volatile(
        "v_and_b32 v25, 0x3f0, %[t16]\n v_mov_b32 v26, 4\n"
        "s_cmp_eq_u32 %[srt], 0\n s_cbranch_scc0 6f\n"
        "s_cmp_ge_u32 %[wv], (" STR(NWARM) "+63)/64\n s_cbranch_scc0 13f\n s_waitcnt vmcnt(0)\n 13:\n s_waitcnt vmcnt(1)\n"
        TAGS
        "s_cmp_lg_u32 %[wv], 0\n s_cbranch_scc1 10f\n"
        "v_mov_b32 v28, %[tx]\n v_mov_b32 v29, %[ty]\n s_mov_b64 exec, 1\n"
        "buffer_store_dwordx2 v[28:29], off, %[rw], %[ao] sc1\n s_mov_b64 exec, -1\n"
        "s_mov_b32 s48, 0x20000\n"
        "9:\n"
        "buffer_load_dword v28, off, %[rw], %[ga] sc1\n"
        "s_waitcnt vmcnt(0)\n"
        "v_cmp_ne_u32 vcc, s55, v28\n"
        "s_cbranch_vccz 10f\n"
        "s_sub_u32 s48, s48, 1\n"
        "s_cbranch_scc1 10f\n"
        "s_sleep " STR(GSLEEP) "\n"
        "s_branch 9b\n"
        "10:\n"
        AST(4)
        "s_waitcnt lgkmcnt(0)\n s_barrier\n"
        POLL
        "s_branch 11f\n"
        "6:\n"
        "v_cmp_le_i32 vcc, 0, %[ea]\n"
        "s_movk_i32 s40, 0x3fc\n s_mov_b32 s41, 0x0c050400\n"
        "s_waitcnt lgkmcnt(0)\n s_barrier\n"
        AST(1)
        "buffer_load_dwordx4 v[64:67], %[xa], %[rx], 0 offen\n"
        "buffer_load_dwordx4 v[68:71], %[wa], %[rq], 0 offen\n buffer_load_dwordx4 v[72:75], %[wa], %[rq], 0 offen offset:128\n"
        "v_add_u32 v46, %[mx], v25\n v_add_u32 v47, %[mw], v25\n"
        "s_mov_b64 exec, vcc\n s_cbranch_execz 7f\n"
        "s_waitcnt vmcnt(5)\n"
        ".irp j,0,1,2,3\n v_mul_u32_u24 v[48+\\j], 5350, v[32+\\j]\n .endr\n"
        ".irp j,0,1,2,3\n v_and_b32_sdwa v[48+\\j], s40, v[48+\\j] dst_sel:DWORD dst_unused:UNUSED_PAD src0_sel:DWORD src1_sel:WORD_1\n"
        " ds_add_rtn_u32 v[52+\\j], v[48+\\j], v26 offset:" STR(L_HIST_) "\n .endr\n"
        "s_waitcnt vmcnt(4)\n"
        ".irp j,0,1,2,3\n v_lshlrev_b32 v[32+\\j], 2, v[32+\\j]\n v_perm_b32 v[36+\\j], v[36+\\j], v[32+\\j], s41\n .endr\n"
        "7:\n"
        "s_mov_b64 exec, -1\n"
        "s_waitcnt vmcnt(3)\n"
        TAGS
        "s_waitcnt lgkmcnt(0)\n s_barrier\n"
        AST(2)
        "s_cmp_lg_u32 %[wv], 13\n s_cbranch_scc1 17f\n"
        "ds_read_b128 v[28:31], v25 offset:" STR(L_HIST_) "\n"
        "s_waitcnt lgkmcnt(0)\n"
        "v_add_u32 v57, v28, v29\n v_add_u32 v58, v57, v30\n v_add_u32 v44, v58, v31\n s_nop 1\n"
        "v_add_u32_dpp v45, v44, v44 row_shr:1 row_mask:0xf bank_mask:0xf bound_ctrl:1\n s_nop 1\n"
        "v_add_u32_dpp v45, v45, v45 row_shr:2 row_mask:0xf bank_mask:0xf bound_ctrl:1\n s_nop 1\n"
        "v_add_u32_dpp v45, v45, v45 row_shr:4 row_mask:0xf bank_mask:0xf bound_ctrl:1\n s_nop 1\n"
        "v_add_u32_dpp v45, v45, v45 row_shr:8 row_mask:0xf bank_mask:0xf bound_ctrl:1\n s_nop 1\n"
        "v_add_u32_dpp v45, v45, v45 row_bcast:15 row_mask:0xa bank_mask:0xf\n s_nop 1\n"
        "v_add_u32_dpp v45, v45, v45 row_bcast:31 row_mask:0xc bank_mask:0xf\n"
        "v_sub_u32 v56, v45, v44\n v_add_u32 v59, v56, v58\n v_add_u32 v58, v56, v57\n v_add_u32 v57, v56, v28\n"
        "ds_write_b128 v25, v[56:59] offset:" STR(L_OFFS_) "\n"
        "s_waitcnt lgkmcnt(0)\n s_barrier\n"
        ".irp j,0,1,2,3\n v_lshrrev_b32 v[32+2*\\j], 2, v[56+\\j]\n v_lshl_or_b32 v[32+2*\\j], v[28+\\j], 10, v[32+2*\\j]\n"
        " v_or_b32 v[32+2*\\j], s54, v[32+2*\\j]\n v_mov_b32 v[33+2*\\j], s55\n .endr\n"
        "buffer_store_dwordx4 v[32:35], v25, %[rw], %[to] offen sc1\n"
        "buffer_store_dwordx4 v[36:39], v25, %[rw], %[to] offen offset:1024 sc1\n"
        "s_branch 8f\n"
        "17:\n"
        "s_barrier\n"
        "s_mov_b64 exec, vcc\n s_cbranch_execz 8f\n"
        ".irp j,0,1,2,3\n ds_read_b32 v[48+\\j], v[48+\\j] offset:" STR(L_OFFS_) "\n .endr\n"
        "s_waitcnt lgkmcnt(0)\n"
        ".irp j,0,1,2,3\n v_add_u32 v[48+\\j], v[48+\\j], v[52+\\j]\n ds_write_b32 v[48+\\j], v[36+\\j] offset:" STR(L_SORT_) "\n .endr\n"
        "8:\n"
        "s_mov_b64 exec, -1\n"
        "s_waitcnt lgkmcnt(0)\n s_barrier\n"
        AST(3)
        "s_cmp_ge_u32 %[wv], 13\n s_cbranch_scc1 18f\n"
        "ds_read_b128 v[32:35], %[t16] offset:" STR(L_SORT_) "\n"
        "s_waitcnt lgkmcnt(0)\n"
        "v_or_b32 v32, s56, v32\n v_or_b32 v33, s57, v33\n v_or_b32 v34, s56, v34\n v_or_b32 v35, s57, v35\n"
        "buffer_store_dwordx4 v[32:35], %[eo], %[rw], 0 offen sc1\n"
        "s_waitcnt vmcnt(1)\n s_branch 19f\n"
        "18:\n"
        "s_cmp_eq_u32 %[wv], 13\n s_cbranch_scc1 20f\n s_waitcnt vmcnt(0)\n 20:\n s_waitcnt vmcnt(2)\n"
        "19:\n"
        "ds_write_b128 v46, v[64:67]\n ds_write_b128 v47, v[68:71]\n ds_write_b128 v47, v[72:75] offset:1024\n"
        POLL
        "s_waitcnt lgkmcnt(0)\n s_barrier\n"
        "11:\n"
        "s_mov_b32 %[tlo], s54\n s_mov_b32 %[thi], s55\n s_mov_b32 %[te], s56\n"
        : [tx] "=&s"(tag_x), [ty] "=&s"(tag_y), [tlo] "=&s"(tag_lo), [thi] "=&s"(tag_hi), [te] "=&s"(tag_e), "=&{v[88:89]}"(tw)
        : "{v[32:35]}"(d4), "{v[36:39]}"(s4), "{v[40:41]}"(ep), "{v84}"(wm0), "{v85}"(wm1), "{v86}"(wm2),
          [t16] "v"(t16), [eo] "v"(eo), [ea] "v"(ea), [xa] "v"(xa), [wa] "v"(wa), [pa] "v"(pa),
          [wv] "s"(v), [srt] "s"((int)sorter), [rx] "s"(sx), [rw] "s"(sw), [rq] "s"(sq), [c128] "s"(128),
          [mx] "s"(L_X + 1024 * v), [mw] "s"(L_WQ + 2048 * v), [to] "s"(WS_TAB + c * NB * 8), [ao] "s"(WS_ACK + c * 8),
          [ga] "s"(WS_TAB + c * NB * 8 + tcol + 4)
        : "v25", "v26", "v28", "v29", "v30", "v31", "v44", "v45", "v48", "v49", V10(5), "v46", "v47", "v64", "v65", "v66", "v67", "v68", "v69", "v70", "v71", "v72", "v73", "v74", "v75", "s48", "s54", "s55", "s56", "s57", CLOB_S CLOB_ST);
    tag_x = __builtin_amdgcn_readfirstlane(tag_x);
    tag_y = __builtin_amdgcn_readfirstlane(tag_y);
    tag_lo = __builtin_amdgcn_readfirstlane(tag_lo);
    tag_hi = __builtin_amdgcn_readfirstlane(tag_hi);
    tag_e = __builtin_amdgcn_readfirstlane(tag_e);

    unsigned cnt_v, over;
    asm volatile(
        "v_mov_b32 v26, 4\n v_bfrev_b32 v27, 1\n"
        "s_mov_b32 s48, 0x20000\n"
        WTR_(96, 0, "tq", 0, 4096) BRD_(0, "gb", 512, 4096) WTR_(96, 1, "tq", 8192, 4096) BRD_(1, "gb", 512, 4096)
        WTR_(96, 2, "tq", 16384, 4096) BRD_(2, "gb", 512, 4096) WTR_(96, 3, "tq", 24576, 4096) BRD_(3, "gb", 512, 4096)
        GMF_(96, 0, 12, "0", "0") GMF(96, 1, 8, D01) GMF(96, 2, 4, D01) GMF(96, 3, 0, D01)
        "s_nop 6\n"
        DCVT
        "ds_write_b64 %[qw], v[112:113]\n ds_write_b64 %[qw], v[116:117] offset:" STR(16*288+16) "\n"
        AST(4)
        "s_waitcnt vmcnt(0)\n"
        AST(5)
        "1:\n"
        "v_xor_b32 v88, %[tlo], v88\n"
        "v_cmp_ne_u32_e64 s[42:43], %[thi], v89\n"
        "v_cmp_le_u32 vcc, 0x1000000, v88\n"
        "s_or_b64 exec, vcc, s[42:43]\n"
        "s_cbranch_execz 2f\n"
        "s_sub_u32 s48, s48, 1\n"
        "s_cbranch_scc1 2f\n"
        "s_sleep " STR(PSLEEP) "\n"
        POLL
        "s_waitcnt vmcnt(0)\n"
        "s_branch 1b\n"
        "2:\n"
        "s_mov_b64 exec, -1\n"
        AST(6)
        "v_and_b32 v89, 3, v88\n v_bfe_u32 v25, v88, 2, 10\n v_bfe_u32 v24, v88, 12, 12\n"
        "v_sub_u32 v88, %[j4], v89\n"
        "v_lshl_add_u32 v25, v25, 4, %[eb]\n"
        "4:\n"
        "v_add_u32 v123, 3, v88\n v_max_i32 v120, 0, v88\n"
        "v_cmp_lt_u32_e64 s[50:51], v120, v24\n"
        "v_add_u32 v121, 1, v88\n v_add_u32 v122, 2, v88\n"
        "v_cndmask_b32_e64 v28, v27, v25, s[50:51]\n"
        "buffer_load_dwordx4 v[28:31], v28, %[rw], 0 offen sc1\n"
        "v_cmp_lt_u32_e64 s[40:41], v88, v24\n v_cmp_lt_u32_e64 s[42:43], v121, v24\n"
        "v_cmp_lt_u32_e64 s[44:45], v122, v24\n v_cmp_lt_u32_e64 s[46:47], v123, v24\n"
        "v_add_u32 v88, 32, v88\n"

        "s_mov_b32 s48, 0x20000\n"
        "v_cmp_lt_i32_e64 s[52:53], v88, v24\n"
        "3:\n"
        "s_waitcnt vmcnt(0)\n"
        "v_xor_b32 v28, %[te], v28\n v_xor_b32 v29, %[tf], v29\n v_xor_b32 v30, %[te], v30\n v_xor_b32 v31, %[tf], v31\n"
        "v_or_b32 v120, v28, v29\n v_or3_b32 v120, v120, v30, v31\n v_and_b32 v120, 0xffc00003, v120\n"
        "v_cmp_ne_u32 vcc, 0, v120\n"
        "s_and_b64 exec, vcc, s[50:51]\n"
        "s_cbranch_execz 14f\n"
        "s_sub_u32 s48, s48, 1\n"
        "s_cbranch_scc1 14f\n"
        "s_sleep " STR(PSLEEP) "\n"
        "buffer_load_dwordx4 v[28:31], v25, %[rw], 0 offen sc1\n"
        "s_branch 3b\n"
        "14:\n"
        AST(7)
        "s_mov_b64 exec, s[40:41]\n v_and_b32 v120, 60, v28\n ds_add_rtn_u32 v120, v120, v26\n"
        "s_mov_b64 exec, s[42:43]\n v_and_b32 v121, 60, v29\n ds_add_rtn_u32 v121, v121, v26\n"
        "s_mov_b64 exec, s[44:45]\n v_and_b32 v122, 60, v30\n ds_add_rtn_u32 v122, v122, v26\n"
        "s_mov_b64 exec, s[46:47]\n v_and_b32 v123, 60, v31\n ds_add_rtn_u32 v123, v123, v26\n"
        "s_waitcnt lgkmcnt(0)\n"
        "v_min_u32 v123, %[lmax], v123\n ds_write_b32 v123, v31\n"
        "s_mov_b64 exec, s[44:45]\n v_min_u32 v122, %[lmax], v122\n ds_write_b32 v122, v30\n"
        "s_mov_b64 exec, s[42:43]\n v_min_u32 v121, %[lmax], v121\n ds_write_b32 v121, v29\n"
        "s_mov_b64 exec, s[40:41]\n v_min_u32 v120, %[lmax], v120\n ds_write_b32 v120, v28\n"
        "s_mov_b64 exec, -1\n"
        "s_cmp_eq_u64 s[52:53], 0\n"
        "s_waitcnt lgkmcnt(0)\n"
        "s_cbranch_scc1 5f\n"
        "v_add_u32 v25, 0x80, v25\n"
        "s_branch 4b\n"
        "5:\n"
        AST(8)
        "s_barrier\n"
        "ds_read_b32 v24, %[ca]\n"
        "ds_read2_b32 v[28:29], %[lp] offset0:0 offset1:8\n"
        "ds_read2_b32 v[30:31], %[lp] offset0:16 offset1:24\n"
        "s_mov_b32 m0, %[buf]\n"
        "s_waitcnt lgkmcnt(2)\n"
        "v_readlane_b32 %[cnt], v24, %[wv]\n"
        "s_waitcnt lgkmcnt(0)\n"
        "v_and_or_b32 v28, v28, %[msk], %[d0]\n v_and_or_b32 v29, v29, %[msk], %[d0]\n"
        "v_and_or_b32 v30, v30, %[msk], %[d0]\n v_and_or_b32 v31, v31, %[msk], %[d0]\n"
        "s_sub_i32 %[cnt], %[cnt], %[lb4]\n"
        "s_max_i32 %[cnt], %[cnt], 0\n"
        "s_lshr_b32 s40, %[cnt], 7\n"
        PRIO("s40")
        DMA_BLK("v28") "s_nop 0\n" DMA_BLK("v29") "s_nop 0\n" DMA_BLK("v30") "s_nop 0\n" DMA_BLK("v31")
        "v_cmp_gt_u32 vcc, v24, %[lim]\n"
        "s_or_b32 %[ov], vcc_lo, vcc_hi\n"
        AST(9)
        : [cnt] "=&s"(cnt_v), [ov] "=&s"(over)
        : "{v[88:89]}"(tw), "{v84}"(wm0), "{v85}"(wm1), "{v86}"(wm2),
          [pa] "v"(pa), [j4] "v"(j4), [eb] "v"(eb), [gb] "v"(gb), [tq] "v"(tq), [qw] "v"(qw), [lp] "v"(lpp), [ca] "v"(ca), [lim] "v"(lim), [d0] "v"(d0), [tlo] "s"(tag_lo), [thi] "s"(tag_hi), [te] "s"(tag_e), [tf] "s"(tag_e ^ TAGM),
          [wv] "s"(v), [srt] "s"((int)sorter), [rx] "s"(sx), [rw] "s"(sw), [c128] "s"(128), [msk] "s"(0x003fff00),
          [lb4] "s"(lbase + 4), [buf] "s"(buf), [lmax] "s"(L_QS - 4)
        : "v24", CLOB_V2, "s48", "s50", "s51", "s52", "s53", CLOB_S CLOB_ST);
    asm volatile("" : "+v"(t));
    const unsigned dl0 = (v - b * NPB) & 63;
    const unsigned qrow = L_QS + (dl0 & 15) * QP + (((dl0 >> 4) + (m >> 2)) & 3) * QT;
    const unsigned np = lbase + (16 * (bq >> 1) + 4 * g + 2 * (bq & 1)) * 4, s0 = buf + srl, t0 = buf + 2048 * (g >> 1) + trl, t1 = t0 ^ 32;
    const unsigned av = (m >> 2) << 6 | v << 2, qa = qrow + 16 * g, fa = qrow + 8 * g + 64 * bq;
    unsigned gb2 = L_QS + prow + g * 16, ow = L_OUT + prow + nt * 32 + g * 8, orr = L_OUT + (t >> 8) * QT + ((t >> 4) & 15) * QP + 16 * (t & 15);
    unsigned oa = t < NPB * 16 ? b * NPB * 256 + t * 16 : OOB, tw0 = L_X + 8192 * (g >> 1) + trn, tw1 = tw0 + 65536;
    asm volatile("" : "+v"(gb2), "+v"(ow), "+v"(orr), "+v"(oa), "+v"(tw0), "+v"(tw1));
    auto rx = __builtin_amdgcn_make_buffer_rsrc((void*)x, 0, NN * 256, 0x00020000);
    auto rw = __builtin_amdgcn_make_buffer_rsrc((void*)ws, 0, WS_END, 0x00020000);
    if (w == NB - 1 && v == 15) {
        for (int i = 0; i < 0x20000; i++) {
            const u32x4 a = __builtin_amdgcn_raw_buffer_load_b128(rw, WS_ACK + lane * 16, 0, SC1);
            if (__ballot(a.x != tag_x || a.y != tag_y || a.z != tag_x || a.w != tag_y) == 0) break;
            __builtin_amdgcn_s_sleep(8);
        }
        if (lane == 0) __builtin_amdgcn_raw_buffer_store_b64(u32x2{tag_x, tag_y}, rw, WS_EPOCH, 0, SC1);
    }
    if (over == 0) {
        unsigned left = cnt_v >> 7, cur = 0, nxt = cnt_v & ~127u, seq = 128, rows = cnt_v & 127;
        asm volatile(
            "ds_read_b128 v[96:99], %[qa]\n ds_read_b128 v[100:103], %[qa] offset:64\n"
            "ds_read_b128 v[104:107], %[qa] offset:128\n ds_read_b128 v[108:111], %[qa] offset:192\n"
            ".irp i,64,65,66,67,68,69,70,71,72,73,74,75,76,77,78,79,80,81,82,83,84,85,86,87,88,89,90,91,92,93,94,95\n v_mov_b32 v\\i, 0\n .endr\n"
            "s_mov_b32 s40, 0xaaaaaaaa\n s_mov_b32 s41, s40\n s_mov_b32 s42, 0xcccccccc\n s_mov_b32 s43, s42\n"
            "s_sub_i32 s44, %[left], 1\n"
            PRIO("s44")
            "1:\n"
            "v_add_u32 v30, %[cur], %[np]\n v_add_u32 v31, %[nxt], %[lp]\n"
            "ds_read_b64 v[28:29], v30\n"
            "ds_read2_b32 v[24:25], v31 offset0:0 offset1:8\n"
            "ds_read2_b32 v[26:27], v31 offset0:16 offset1:24\n"
            "s_mov_b32 %[cur], %[nxt]\n s_mov_b32 %[nxt], %[seq]\n s_add_u32 %[seq], %[seq], 0x80\n"
            "s_waitcnt vmcnt(0)\n"
            SRD_(0) SRD_(1) SRD_(2) SRD_(3) SRD_(4) SRD_(5) SRD_(6) SRD_(7)
            SMF_(0, 7, "v[112:115]", "0") TRD_(0, "t0")
            SMF_(1, 8, "v[116:119]", "0") TRD_(1, "t1")
            SMF_(2, 9, "v[112:115]", "v[112:115]") TRD_(2, "t0")
            SMF_(3, 10, "v[116:119]", "v[116:119]") TRD_(3, "t1")
            SMF_(4, 11, "v[112:115]", "v[112:115]") TRD_(4, "t0")
            SMF_(5, 12, "v[116:119]", "v[116:119]") TRD_(5, "t1")
            SMF_(6, 13, "v[112:115]", "v[112:115]") TRD_(6, "t0")
            SMF_(7, 14, "v[116:119]", "v[116:119]") TRD_(7, "t1")
            "v_and_or_b32 v24, v24, %[msk], %[d0]\n v_and_or_b32 v25, v25, %[msk], %[d0]\n"
            "v_and_or_b32 v26, v26, %[msk], %[d0]\n v_and_or_b32 v27, v27, %[msk], %[d0]\n"
            "v_cmp_eq_u32_sdwa s[44:45], v28, %[av] src0_sel:BYTE_0 src1_sel:DWORD\n"
            "v_cmp_eq_u32_sdwa s[46:47], v29, %[av] src0_sel:BYTE_0 src1_sel:DWORD\n"
            "v_cndmask_b32_e64 v28, v112, v114, s[40:41]\n v_cndmask_b32_e64 v29, v113, v115, s[40:41]\n"
            "v_cndmask_b32_e64 v30, v116, v118, s[40:41]\n v_cndmask_b32_e64 v31, v117, v119, s[40:41]\n"
            "v_cndmask_b32_e64 v28, v28, v30, s[42:43]\n v_cndmask_b32_e64 v29, v29, v31, s[42:43]\n"
            "v_mul_f32 v28, 0xbe0293ee, v28\n v_mul_f32 v29, 0xbe0293ee, v29\n"
            "v_exp_f32 v28, v28\n v_exp_f32 v29, v29\n"
            "s_mov_b32 m0, %[buf]\n"
            "s_cmp_eq_u32 %[left], 0\n"
            "s_waitcnt lgkmcnt(0)\n"
            "s_cbranch_scc1 3f\n"
            DMA_BLK("v24") "s_cmp_lt_u32 %[rows], 32\n s_cbranch_scc1 9f\n"
            DMA_BLK("v25") "s_cmp_lt_u32 %[rows], 64\n s_cbranch_scc1 9f\n"
            DMA_BLK("v26") "s_cmp_lt_u32 %[rows], 96\n s_cbranch_scc1 9f\n"
            DMA_BLK("v27")
            "9:\n"
            "s_movk_i32 %[rows], 0x7f\n"
            "s_sub_i32 vcc_lo, %[left], 2\n"
            PRIO("vcc_lo")
            "2:\n"
            "v_add_f32 v28, 1.0, v28\n v_add_f32 v29, 1.0, v29\n"
            "v_rcp_f32 v28, v28\n v_rcp_f32 v29, v29\n"
            "s_nop 0\n"
            "v_cndmask_b32_e64 v28, 0, v28, s[44:45]\n v_cndmask_b32_e64 v29, 0, v29, s[46:47]\n"
            "v_cvt_pk_bf16_f32 v28, v28, v29\n"
            "s_cmp_lg_u32 %[left], 0\n s_nop 0\n"
            "v_mov_b32_dpp v120, v28 quad_perm:[0,0,0,0] row_mask:0xf bank_mask:0xf\n"
            "v_mov_b32_dpp v121, v28 quad_perm:[1,1,1,1] row_mask:0xf bank_mask:0xf\n"
            "v_mov_b32_dpp v122, v28 quad_perm:[2,2,2,2] row_mask:0xf bank_mask:0xf\n"
            "v_mov_b32_dpp v123, v28 quad_perm:[3,3,3,3] row_mask:0xf bank_mask:0xf\n"
            "s_nop 1\n"
            PMF_(0) PMF_(1) PMF_(2) PMF_(3) PMF_(4) PMF_(5) PMF_(6) PMF_(7)
            "s_cbranch_scc0 4f\n"
            "s_sub_u32 %[left], %[left], 1\n"
            "s_branch 1b\n"
            "3:\n"
            DMA2("%[wa]", "%[rv]", "0", "%[c128]")
            "s_branch 2b\n"
            "4:\n"
            "s_nop 1\n"
            CVT_(0) CVT_(1) CVT_(2) CVT_(3) CVT_(4) CVT_(5) CVT_(6) CVT_(7) SEL_(0) SEL_(1) SEL_(2) SEL_(3)
            "s_waitcnt vmcnt(0)\n"
            AST(10)
            "s_barrier\n"
            "ds_write_b64 %[fa], v[32:33]\n ds_write_b64 %[fa], v[34:35] offset:32\n"
            : [left] "+&s"(left), [cur] "+&s"(cur), [nxt] "+&s"(nxt), [seq] "+&s"(seq), [rows] "+&s"(rows)
            : [lp] "v"(lp), [np] "v"(np), [d0] "v"(d0), [s0] "v"(s0), [t0] "v"(t0), [t1] "v"(t1), [av] "v"(av),
              [qa] "v"(qa), [fa] "v"(fa), [wa] "v"(wa), [rv] "s"(sv),
              [buf] "s"(buf), [rx] "s"(sx), [c128] "s"(128), [msk] "s"(0x003fff00)
            : "v24", CLOB_V, CLOB_S CLOB_ST);
    } else {
        float ac[4][2] = {};
        for (int k = 0; k < NC; k++) {
            const unsigned tk = __builtin_amdgcn_readfirstlane(__builtin_amdgcn_raw_buffer_load_b32(rw, WS_TAB + k * NB * 8 + tcol, 0, SC1));
            const unsigned sg = WS_ENT + (k * CHP + (tk & 0xfffu)) * 4;
            for (int i = 0; i < (int)(tk >> 12 & 0xfffu); i++) {
                const unsigned e = __builtin_amdgcn_readfirstlane(__builtin_amdgcn_raw_buffer_load_b32(rw, sg + i * 4, 0, SC1)) & ~TAGM;
                if ((int)(e >> 2 & 15) != v) continue;
                const int sl = e >> 6 & 3;
                const unsigned xv = __builtin_amdgcn_raw_buffer_load_b32(rx, (e & 0x3fff00u) + lane * 4, 0, 0);
                const unsigned qv = *(const unsigned*)(lds + L_QS + (dl0 & 15) * QP + (((dl0 >> 4) + sl) & 3) * QT + lane * 4);
                const float x0 = __builtin_bit_cast(float, xv << 16), x1 = __builtin_bit_cast(float, xv & 0xffff0000u);
                float s = x0 * __builtin_bit_cast(float, qv << 16) + x1 * __builtin_bit_cast(float, qv & 0xffff0000u);
#pragma unroll
                for (int o = 32; o > 0; o >>= 1) s += __shfl_xor(s, o, 64);
                const float al = (float)(__bf16)__builtin_amdgcn_rcpf(1.0f + __builtin_amdgcn_exp2f(s * -0.12751743f));
#pragma unroll
                for (int a = 0; a < 4; a++) {
                    const float aa = sl == a ? al : 0.f;
                    ac[a][0] += aa * x0;
                    ac[a][1] += aa * x1;
                }
            }
        }
        asm volatile("s_waitcnt vmcnt(0)\n s_mov_b32 m0, %[buf]\n s_nop 0\n" DMA2("%[wa]", "%[rv]", "0", "%[c128]") "s_waitcnt vmcnt(0)\n s_barrier"
                     :: [wa] "v"(wa), [buf] "s"(buf), [rv] "s"(sv), [c128] "s"(128) : "scc", "memory");
#pragma unroll
        for (int a = 0; a < 4; a++) *(unsigned*)(lds + L_QS + (dl0 & 15) * QP + (((dl0 >> 4) + a) & 3) * QT + lane * 4) = pk_bf16(ac[a][0], ac[a][1]);
    }
    asm volatile(
        WTR_(96, 0, "tw0", 0, 16384) WTR_(96, 1, "tw0", 32768, 16384) WTR_(96, 2, "tw1", 0, 16384) WTR_(96, 3, "tw1", 32768, 16384)
        "s_waitcnt lgkmcnt(0)\n"
        "s_barrier\n"
        AST(11)
        BRD_(0, "gb", 64, 16*288+16) BRD_(1, "gb", 64, 16*288+16) BRD_(2, "gb", 64, 16*288+16) BRD_(3, "gb", 64, 16*288+16)
        GMF_(96, 0, 6, "0", "0") GMF(96, 1, 4, D01) GMF(96, 2, 2, D01) GMF(96, 3, 0, D01)
        "s_nop 6\n"
        DCVT
        "ds_write_b64 %[ow], v[112:113]\n ds_write_b64 %[ow], v[116:117] offset:" STR(16*288+16) "\n"
        "s_waitcnt lgkmcnt(0)\n"
        AST(12)
        "s_barrier\n"
        "s_cmp_ge_u32 %[wv], 13\n s_cbranch_scc1 1f\n"
        "ds_read_b128 v[28:31], %[orr]\n"
        "s_waitcnt lgkmcnt(0)\n"
        "buffer_store_dwordx4 v[28:31], %[oa], %[ro], 0 offen" OUTPOL "\n"
        "1:\n"
        AST(13)
        :: [gb] "v"(gb2), [ow] "v"(ow), [orr] "v"(orr), [oa] "v"(oa), [ro] "s"(make_srd(out, NN * 256)), [wv] "s"(v), [tw0] "v"(tw0), [tw1] "v"(tw1)
        : "v24", CLOB_V2, CLOB_S CLOB_ST);
}

#define ST1

extern "C" void kernel_launch(void* const* d_in, const int* in_sizes, int n_in, void* d_out, int out_size,
                              void* d_ws, size_t ws_size, hipStream_t stream) {
    featgraph_fused<<<NB, 1024, 0, stream>>>(d_in[0], (const int*)d_in[1], (const int*)d_in[2], d_in[3], d_in[4],
                                             (char*)d_ws, (char*)d_out ST1);
}
```
